```python
import torch

def _softmax(x, axis=-1):
    return torch.softmax(x, dim=axis)
def _gelu(x):
    return torch.nn.functional.gelu(x, approximate="tanh")
def _silu(x):
    return torch.nn.functional.silu(x)
def _sigmoid(x):
    return torch.sigmoid(x)
def _layernorm(x, axis=-1, eps=1e-5):
    m = x.mean(dim=axis, keepdim=True)
    v = x.var(dim=axis, keepdim=True, unbiased=False)
    return (x - m) / torch.sqrt(v + eps)
def _scatter_add(tgt, idx, src, axis=0):
    return tgt.clone().index_add_(axis, idx, src)
def _segment_sum(src, seg, n):
    out = torch.zeros((n,) + src.shape[1:], dtype=src.dtype)
    return out.index_add_(0, seg, src)
def _pad(x, pw):
    flat = tuple(v for pair in reversed(pw) for v in pair)
    return torch.nn.functional.pad(x, flat)

kernel_name = 'gated_mix__sliding_block_cached__mixer_channel_mlp__decode'

def setup_inputs():

    _w = (lambda a: (lambda t: t.to(torch.bfloat16) if t.is_floating_point() else t)(torch.as_tensor(a)))
    def _A_setup():
        B, Sq, D = 4, 1, 512
        Hq, Hkv, Sk, Dh = 32, 8, 4096, 128
        assert Hq % Hkv == 0, (Hq, Hkv)
        return {
            "x": _w((lambda *s: torch.randn(*s, dtype=torch.bfloat16) * 0.05)(B, Sq, D)),
            "k_cache": _w((lambda *s: torch.randn(*s, dtype=torch.bfloat16) * 6.4)(B, Hkv, Sk, Dh)),
            "v_cache": _w((lambda *s: torch.randn(*s, dtype=torch.bfloat16) * 0.4)(B, Hkv, Sk, Dh)),
            "wq": _w((lambda *s: torch.randn(*s, dtype=torch.bfloat16) * 6.4)(D, Hq * Dh) * (D ** -0.5)),
            "wo": _w((lambda *s: torch.randn(*s, dtype=torch.bfloat16) * 0.4)(Hq * Dh, D) * ((Hq * Dh) ** -0.5)),
        }
    _A_in = _A_setup()

    def _B_setup():
        B, S, D, F = 4, 1, 512, 1792
        return {
            "x": _w((lambda *s: torch.randn(*s, dtype=torch.bfloat16) * 0.05)(B, S, D)),
            "w1": _w((lambda *s: torch.randn(*s, dtype=torch.bfloat16) * 0.8)(D, F) * (D ** -0.5)),
            "b1": _w((lambda *s: torch.randn(*s, dtype=torch.bfloat16) * 0.8)(F)),
            "w2": _w((lambda *s: torch.randn(*s, dtype=torch.bfloat16) * 0.05)(F, D) * (F ** -0.5)),
            "b2": _w((lambda *s: torch.randn(*s, dtype=torch.bfloat16) * 0.05)(D)),
        }
    _B_in = _B_setup()

    return {
        "A_x": _A_in["x"],
        "A_k_cache": _A_in["k_cache"],
        "A_v_cache": _A_in["v_cache"],
        "A_wq": _A_in["wq"],
        "A_wo": _A_in["wo"],
        "B_x": _B_in["x"],
        "B_w1": _B_in["w1"],
        "B_b1": _B_in["b1"],
        "B_w2": _B_in["w2"],
        "B_b2": _B_in["b2"],
        "gate_w": _w((lambda *s: torch.randn(*s, dtype=torch.bfloat16) * 0.05)(512) * 16.0),
    }

def reference(A_x, A_k_cache, A_v_cache, A_wq, A_wo, B_x, B_w1, B_b1, B_w2, B_b2, gate_w):
    def _A_ref(x, k_cache, v_cache, wq, wo):
        B, Sq, D = x.shape
        Hq, Dh = 32, 128
        Hkv, Sk = k_cache.shape[1], k_cache.shape[2]
        W = min(4096, max(256, Sk // 4))
        rep = Hq // Hkv
        q = (lambda x, *d: x.permute(*d))((x @ wq).reshape(B, Sq, Hq, Dh), 0, 2, 1, 3)
        k = (lambda x, n, axis: torch.repeat_interleave(x, n, dim=axis))(k_cache[:, :, Sk - W:, :], rep, axis=1)
        v = (lambda x, n, axis: torch.repeat_interleave(x, n, dim=axis))(v_cache[:, :, Sk - W:, :], rep, axis=1)
        scores = torch.einsum("bhqd,bhkd->bhqk", q, k) * (Dh ** -0.5)
        p = _softmax(scores, axis=-1)
        o = torch.einsum("bhqk,bhkd->bhqd", p, v)
        return (lambda x, *d: x.permute(*d))(o, 0, 2, 1, 3).reshape(B, Sq, Hq * Dh) @ wo

    def _B_ref(x, w1, b1, w2, b2):
        y = _layernorm(x, axis=-1)
        h = _gelu(torch.einsum("bsd,df->bsf", y, w1) + b1)
        return torch.einsum("bsf,fd->bsd", h, w2) + b2

    x = A_x
    g = _sigmoid((x * gate_w).sum(-1, keepdims=True))
    a = _A_ref(x, A_k_cache, A_v_cache, A_wq, A_wo)
    b = _B_ref(x, B_w1, B_b1, B_w2, B_b2)
    return g * a + (1 - g) * b
```

```cpp
#include <hip/hip_runtime.h>
#include <hip/hip_bf16.h>
#include <stdint.h>

typedef unsigned short u16;
__device__ __forceinline__ float bf2f(u16 v) { return __uint_as_float(((unsigned)v) << 16); }
__device__ __forceinline__ u16 f2bf(float f) {
    __hip_bfloat16 b = __float2bfloat16(f);
    return __builtin_bit_cast(u16, b);
}
__device__ __forceinline__ float rbf(float f) { return bf2f(f2bf(f)); }

__global__ void k1(const u16* x, const u16* wq, const u16* w1, const u16* b1, float* q, float* h) {
    __shared__ float xs[4][512];
    __shared__ float ys[4][512];
    __shared__ float st[4][2];
    int tid = threadIdx.x;
    for (int i = tid; i < 2048; i += blockDim.x) xs[i / 512][i % 512] = bf2f(x[i]);
    __syncthreads();
    if (tid < 4) {
        float s = 0.f;
        for (int i = 0; i < 512; ++i) s += xs[tid][i];
        float m = s / 512.f;
        float v = 0.f;
        for (int i = 0; i < 512; ++i) { float d = xs[tid][i] - m; v += d * d; }
        v /= 512.f;
        st[tid][0] = rbf(m);
        st[tid][1] = rbf(sqrtf(rbf(rbf(v) + 1e-5f)));
    }
    __syncthreads();
    for (int i = tid; i < 2048; i += blockDim.x) { int b = i / 512; ys[b][i % 512] = rbf(rbf(xs[b][i % 512] - st[b][0]) / st[b][1]); }
    __syncthreads();
    int n = blockIdx.x * blockDim.x + tid;
    if (n < 4096) {
        float a[4] = {0, 0, 0, 0};
        for (int k = 0; k < 512; ++k) { float w = bf2f(wq[(size_t)k * 4096 + n]); for (int b = 0; b < 4; ++b) a[b] += xs[b][k] * w; }
        for (int b = 0; b < 4; ++b) q[b * 4096 + n] = rbf(a[b]);
    } else if (n < 4096 + 1792) {
        int f = n - 4096;
        float a[4] = {0, 0, 0, 0};
        for (int k = 0; k < 512; ++k) { float w = bf2f(w1[(size_t)k * 1792 + f]); for (int b = 0; b < 4; ++b) a[b] += ys[b][k] * w; }
        for (int b = 0; b < 4; ++b) {
            float z = rbf(rbf(a[b]) + bf2f(b1[f]));
            float inner = 0.7978845608028654f * (z + 0.044715f * z * z * z);
            float g = 0.5f * z * (1.f + tanhf(inner));
            h[b * 1792 + f] = rbf(g);
        }
    }
}

__global__ void k2(const u16* kc, const u16* vc, const float* q, float* o) {
    __shared__ float qs[128];
    __shared__ float p[1024];
    __shared__ float red[256];
    int b = blockIdx.x / 32, hh = blockIdx.x % 32, kvh = hh / 4;
    int tid = threadIdx.x;
    if (tid < 128) qs[tid] = q[b * 4096 + hh * 128 + tid];
    __syncthreads();
    const u16* kb = kc + ((size_t)(b * 8 + kvh) * 4096 + 3072) * 128;
    const u16* vb = vc + ((size_t)(b * 8 + kvh) * 4096 + 3072) * 128;
    float mx = -INFINITY;
    for (int j = tid; j < 1024; j += 256) {
        float s = 0.f;
        for (int d = 0; d < 128; ++d) s += qs[d] * bf2f(kb[(size_t)j * 128 + d]);
        s = rbf(rbf(s) * 0.08838834764831845f);
        p[j] = s; mx = fmaxf(mx, s);
    }
    red[tid] = mx; __syncthreads();
    for (int s = 128; s > 0; s >>= 1) { if (tid < s) red[tid] = fmaxf(red[tid], red[tid + s]); __syncthreads(); }
    mx = red[0]; __syncthreads();
    float sum = 0.f;
    for (int j = tid; j < 1024; j += 256) { float e = expf(p[j] - mx); p[j] = e; sum += e; }
    red[tid] = sum; __syncthreads();
    for (int s = 128; s > 0; s >>= 1) { if (tid < s) red[tid] += red[tid + s]; __syncthreads(); }
    sum = red[0]; __syncthreads();
    for (int j = tid; j < 1024; j += 256) p[j] = rbf(p[j] / sum);
    __syncthreads();
    if (tid < 128) {
        float a = 0.f;
        for (int j = 0; j < 1024; ++j) a += p[j] * bf2f(vb[(size_t)j * 128 + tid]);
        o[b * 4096 + hh * 128 + tid] = rbf(a);
    }
}

__global__ void k3(const u16* x, const u16* gw, const u16* wo, const u16* w2, const u16* b2, const float* o, const float* h, u16* out) {
    int idx = blockIdx.x * blockDim.x + threadIdx.x;
    int b = idx / 512, n = idx % 512;
    float gs = 0.f;
    for (int d = 0; d < 512; ++d) gs += rbf(bf2f(x[b * 512 + d]) * bf2f(gw[d]));
    gs = rbf(gs);
    float g = rbf(1.f / (1.f + expf(-gs)));
    float a = 0.f;
    for (int k = 0; k < 4096; ++k) a += o[b * 4096 + k] * bf2f(wo[(size_t)k * 512 + n]);
    a = rbf(a);
    float c = 0.f;
    for (int k = 0; k < 1792; ++k) c += h[b * 1792 + k] * bf2f(w2[(size_t)k * 512 + n]);
    c = rbf(rbf(c) + bf2f(b2[n]));
    float r = rbf(rbf(g * a) + rbf(rbf(1.f - g) * c));
    out[idx] = f2bf(r);
}

extern "C" void kernel_launch(void* const* d_in, const int* in_sizes, int n_in,
                              void* d_out, int out_size, void* d_ws, size_t ws_size,
                              hipStream_t stream) {
    const u16* x = (const u16*)d_in[0];
    const u16* kc = (const u16*)d_in[1];
    const u16* vc = (const u16*)d_in[2];
    const u16* wq = (const u16*)d_in[3];
    const u16* wo = (const u16*)d_in[4];
    const u16* w1 = (const u16*)d_in[6];
    const u16* b1 = (const u16*)d_in[7];
    const u16* w2 = (const u16*)d_in[8];
    const u16* b2 = (const u16*)d_in[9];
    const u16* gw = (const u16*)d_in[10];
    float* q = (float*)d_ws;
    float* h = q + 4 * 4096;
    float* o = h + 4 * 1792;
    k1<<<(4096 + 1792 + 63) / 64, 64, 0, stream>>>(x, wq, w1, b1, q, h);
    k2<<<128, 256, 0, stream>>>(kc, vc, q, o);
    k3<<<2048 / 64, 64, 0, stream>>>(x, gw, wo, w2, b2, o, h, (u16*)d_out);
}
```

```cpp
#include <hip/hip_runtime.h>
#include <hip/hip_bf16.h>
#include <stdint.h>

typedef unsigned short u16;
typedef unsigned int u32;
typedef u32 u32x2 __attribute__((ext_vector_type(2)));
typedef u32 u32x4 __attribute__((ext_vector_type(4)));
typedef float f32x2 __attribute__((ext_vector_type(2)));
typedef float f32x4 __attribute__((ext_vector_type(4)));
typedef short s16x4 __attribute__((ext_vector_type(4)));
typedef __bf16 bf16x8 __attribute__((ext_vector_type(8)));
typedef __bf16 bf16x2 __attribute__((ext_vector_type(2)));

#define USE_NT 1

constexpr int E_OFF = 0;
constexpr int GQ_OFF = 4096;
constexpr int GH_OFF = GQ_OFF + 65536;
constexpr int GML_OFF = GH_OFF + 32768;
constexpr int GO_OFF = GML_OFF + 16384;
constexpr int G3_OFF = GO_OFF + 1048576;
constexpr int WS_USED = G3_OFF + 4 * 64 * 96 * 64;

constexpr int L_QP = 0;
constexpr int L_HP = L_QP + 2048;
constexpr int L_YS = L_HP + 2048;
constexpr int L_CNT = L_YS + 64;
constexpr int L_QS = L_CNT + 64;
constexpr int L_ML = L_QS + 1024;
constexpr int L_OP = L_ML + 256;
constexpr int L_ST = L_OP + 16384;
constexpr int L_STML = L_ST + 8192;
constexpr int L_WP = L_STML + 1024;
constexpr int L_END = L_WP + 16384;
constexpr int LDS_BYTES = 84 * 1024;
static_assert(L_END <= LDS_BYTES, "lds");

constexpr unsigned SPIN_CAP = 1u << 18;
constexpr float LOG2E = 1.4426950408889634f;

__device__ __forceinline__ float bf_lo(u32 v) { return __uint_as_float(v << 16); }
__device__ __forceinline__ float bf_hi(u32 v) { return __uint_as_float(v & 0xffff0000u); }
__device__ __forceinline__ u32 pack_bf16(float lo, float hi) {
    bf16x2 v = {(__bf16)lo, (__bf16)hi};
    return __builtin_bit_cast(u32, v);
}
__device__ __forceinline__ float rbf(float f) { return bf_lo((u32)__builtin_bit_cast(u16, (__bf16)f)); }

template <int CTRL> __device__ __forceinline__ float dppf(float x) {
    return __builtin_bit_cast(float, __builtin_amdgcn_mov_dpp(__builtin_bit_cast(int, x), CTRL, 0xf, 0xf, true));
}
constexpr int XOR1 = 0xB1, XOR2 = 0x4E, ROR4 = 0x124, ROR8 = 0x128;
__device__ __forceinline__ float quad_sum(float x) { x += dppf<XOR1>(x); x += dppf<XOR2>(x); return x; }
__device__ __forceinline__ float quad_max(float x) { x = fmaxf(x, dppf<XOR1>(x)); x = fmaxf(x, dppf<XOR2>(x)); return x; }
__device__ __forceinline__ float ror_sum(float x) { x += dppf<ROR8>(x); x += dppf<ROR4>(x); return x; }
__device__ __forceinline__ float xrow_max(float x) {
    auto s = __builtin_amdgcn_permlane16_swap(__float_as_uint(x), __float_as_uint(x), false, false);
    x = fmaxf(__uint_as_float(s[0]), __uint_as_float(s[1]));
    auto t = __builtin_amdgcn_permlane32_swap(__float_as_uint(x), __float_as_uint(x), false, false);
    return fmaxf(__uint_as_float(t[0]), __uint_as_float(t[1]));
}
__device__ __forceinline__ float xrow_sum(float x) {
    auto s = __builtin_amdgcn_permlane16_swap(__float_as_uint(x), __float_as_uint(x), false, false);
    x = __uint_as_float(s[0]) + __uint_as_float(s[1]);
    auto t = __builtin_amdgcn_permlane32_swap(__float_as_uint(x), __float_as_uint(x), false, false);
    return __uint_as_float(t[0]) + __uint_as_float(t[1]);
}
__device__ __forceinline__ float wave_sum(float x) { return xrow_sum(ror_sum(quad_sum(x))); }

__device__ __forceinline__ u32x4 ld16(const u16* p) {
    return __builtin_nontemporal_load(reinterpret_cast<const u32x4*>(p));
}
__device__ __forceinline__ u32x2 ld8(const u16* p) { return *reinterpret_cast<const u32x2*>(p); }

__device__ __forceinline__ u32 plo(u32 a, u32 b) { return __builtin_amdgcn_perm(b, a, 0x05040100u); }
__device__ __forceinline__ u32 phi(u32 a, u32 b) { return __builtin_amdgcn_perm(b, a, 0x07060302u); }

__device__ __forceinline__ f32x4 mfma16(u32x2 a, u32x2 b, f32x4 c) {
    return __builtin_amdgcn_mfma_f32_16x16x16bf16_1k(__builtin_bit_cast(s16x4, a), __builtin_bit_cast(s16x4, b), c, 0, 0, 0);
}
__device__ __forceinline__ f32x4 mfma32(u32x4 a, u32x4 b, f32x4 c) {
    return __builtin_amdgcn_mfma_f32_16x16x32_bf16(__builtin_bit_cast(bf16x8, a), __builtin_bit_cast(bf16x8, b), c, 0, 0, 0);
}
__device__ __forceinline__ float sel4(f32x4 d, int s) {
    float lo = (s & 1) ? d[1] : d[0], hi = (s & 1) ? d[3] : d[2];
    return (s & 2) ? hi : lo;
}
#define SC1 16
#define CBAR() asm volatile("" ::: "memory")
#define KEEP4(v) asm volatile("" : "+v"((v).x), "+v"((v).y), "+v"((v).z), "+v"((v).w))
#define LDS_FENCE() asm volatile("s_waitcnt lgkmcnt(0)" ::: "memory")

#define TS(k)
#define TSW(k)

__device__ __forceinline__ f32x4 tile16(u32x2 xa, const u32x2 (&wl)[4], int c) {
    f32x4 z = {0.f, 0.f, 0.f, 0.f};
    u32x2 f0 = {plo(wl[0].x, wl[1].x), plo(wl[2].x, wl[3].x)};
    u32x2 f1 = {phi(wl[0].x, wl[1].x), phi(wl[2].x, wl[3].x)};
    u32x2 f2 = {plo(wl[0].y, wl[1].y), plo(wl[2].y, wl[3].y)};
    u32x2 f3 = {phi(wl[0].y, wl[1].y), phi(wl[2].y, wl[3].y)};
    f32x4 d0 = mfma16(xa, f0, z), d1 = mfma16(xa, f1, z), d2 = mfma16(xa, f2, z), d3 = mfma16(xa, f3, z);
    const int sub = c >> 2;
    f32x4 r;
    r[0] = ror_sum(sel4(d0, sub)); r[1] = ror_sum(sel4(d1, sub)); r[2] = ror_sum(sel4(d2, sub)); r[3] = ror_sum(sel4(d3, sub));
    return r;
}

__global__ void __launch_bounds__(576, 3)
fused_decode(const u16* __restrict__ x, const u16* __restrict__ kc, const u16* __restrict__ vc,
             const u16* __restrict__ wq, const u16* __restrict__ wo, const u16* __restrict__ w1,
             const u16* __restrict__ b1, const u16* __restrict__ w2, const u16* __restrict__ b2,
             const u16* __restrict__ gw, u16* __restrict__ out, unsigned char* ws) {
    __shared__ __attribute__((aligned(16))) unsigned char smem[LDS_BYTES];
    const int tid = threadIdx.x, lane = tid & 63;
    const int wave = __builtin_amdgcn_readfirstlane(tid >> 6);
    const int bid = blockIdx.x, j = bid & 7, i = bid >> 3;
    const bool setA = i < 14, setB = (i >= 14) && (i < 28);
    const int g = lane >> 4, c = lane & 15;
    const __amdgpu_buffer_rsrc_t rs = __builtin_amdgcn_make_buffer_rsrc(ws, 0, WS_USED, 0x00020000);
    const int ab = i >> 3, as = i & 7;
    const int rg = 8 * j + (i >> 2), cgp = i & 3;
    const int tb = j * 14 + (i - 14), rg2 = tb >> 2, cg2 = tb & 3;
    u32* const cnt = reinterpret_cast<u32*>(smem + L_CNT);
    TS(0);
    const u32 ev = __builtin_amdgcn_raw_buffer_load_b32(rs, E_OFF, 0, SC1);

    if (wave < 8) {
        const int ra = 64 * wave + (g * 4 + (c & 3)) * 4;
        const int rb = 64 * wave + (g * 4 + (c >> 2)) * 4;
        u32x2 xa = ld8(x + (c >> 2) * 512 + ra);
        u32x2 wl[4], w1l[4];
        {
            const u16* p = wq + (size_t)rb * 4096 + 512 * j + 16 * i + (c & 3) * 4;
#pragma unroll
            for (int t = 0; t < 4; ++t) wl[t] = ld8(p + t * 4096);
        }
        u32 b1v;
        {
            const __amdgpu_buffer_rsrc_t r1 = __builtin_amdgcn_make_buffer_rsrc((void*)w1, 0, 512 * 1792 * 2, 0x00020000);
            const int off = setA ? (rb * 1792 + 16 * (j * 14 + i) + (c & 3) * 4) * 2 : 0x40000000;
#pragma unroll
            for (int t = 0; t < 4; ++t) w1l[t] = __builtin_amdgcn_raw_buffer_load_b64(r1, off + t * 3584, 0, 0);
            const __amdgpu_buffer_rsrc_t rb1 = __builtin_amdgcn_make_buffer_rsrc((void*)b1, 0, 1792 * 2, 0x00020000);
            b1v = __builtin_amdgcn_raw_buffer_load_b32(rb1, setA ? (16 * (j * 14 + i) + 2 * (lane & 7)) * 2 : 0x40000000, 0, 0);
        }
        const size_t kvbase = ((size_t)(ab * 8 + j) * 4096 + 3072 + 128 * as + 16 * wave) * 128;
        const u16* kp = kc + kvbase + ((lane >> 2) & 3) * 128 + (g * 4 + (lane & 3)) * 8;
        const u16* vp = vc + kvbase + lane * 8;
        u32x4 kf[4], vf[4];
        kf[0] = ld16(kp); kf[1] = ld16(kp + 512);
        __builtin_amdgcn_sched_barrier(0);
        __syncthreads();
        __builtin_amdgcn_sched_barrier(0);
        const u32 tag = __builtin_amdgcn_readfirstlane(ev) + 1u;
        {
            f32x4 r = tile16(xa, wl, c);
            if (c < 4) *reinterpret_cast<f32x4*>(smem + L_QP + ((wave * 4 + g) * 16 + c * 4) * 4) = r;
            LDS_FENCE();
            u32 old = 0;
            if (lane == 0) old = __hip_atomic_fetch_add(cnt + 0, 1u, __ATOMIC_RELAXED, __HIP_MEMORY_SCOPE_WORKGROUP);
            old = __builtin_amdgcn_readfirstlane(old);
            TS(1);
            if (old == 7u) {
                CBAR();
                const int b = (lane >> 3) & 3, p = lane & 7;
                float a0 = 0.f, a1 = 0.f;
#pragma unroll
                for (int w = 0; w < 8; ++w) { f32x2 t = *reinterpret_cast<const f32x2*>(smem + L_QP + ((w * 4 + b) * 16 + 2 * p) * 4); a0 += t.x; a1 += t.y; }
                u32x2 gv = {pack_bf16(a0, a1), tag};
                __builtin_amdgcn_raw_buffer_store_b64(gv, rs, (lane < 32) ? GQ_OFF + ((j * 4 + b) * 256 + 8 * i + p) * 8 : 0x40000000, 0, SC1);
                TSW(9);
            }
        }
        kf[2] = ld16(kp + 1024); kf[3] = ld16(kp + 1536);
        __builtin_amdgcn_sched_barrier(0);
        if (setA) {
            while (__hip_atomic_load(cnt + 2, __ATOMIC_RELAXED, __HIP_MEMORY_SCOPE_WORKGROUP) == 0u) __builtin_amdgcn_s_sleep(1);
            CBAR();
            const f32x4 stt = *reinterpret_cast<const f32x4*>(smem + L_YS + (c >> 2) * 16);
            float d[4] = {rbf(bf_lo(xa.x) - stt[0]), rbf(bf_hi(xa.x) - stt[0]), rbf(bf_lo(xa.y) - stt[0]), rbf(bf_hi(xa.y) - stt[0])};
#pragma unroll
            for (int e = 0; e < 4; ++e) { const float q0 = d[e] * stt[2]; d[e] = fmaf(fmaf(-q0, stt[1], d[e]), stt[2], q0); }
            const u32x2 ya = u32x2{pack_bf16(d[0], d[1]), pack_bf16(d[2], d[3])};
            f32x4 r = tile16(ya, w1l, c);
            if (c < 4) *reinterpret_cast<f32x4*>(smem + L_HP + ((wave * 4 + g) * 16 + c * 4) * 4) = r;
            LDS_FENCE();
            u32 old = 0;
            if (lane == 0) old = __hip_atomic_fetch_add(cnt + 1, 1u, __ATOMIC_RELAXED, __HIP_MEMORY_SCOPE_WORKGROUP);
            old = __builtin_amdgcn_readfirstlane(old);
            if (old == 7u) {
                CBAR();
                const int b = (lane >> 3) & 3, p = lane & 7;
                float a0 = 0.f, a1 = 0.f;
#pragma unroll
                for (int w = 0; w < 8; ++w) { f32x2 t = *reinterpret_cast<const f32x2*>(smem + L_HP + ((w * 4 + b) * 16 + 2 * p) * 4); a0 += t.x; a1 += t.y; }
                float z[2] = {rbf(rbf(a0) + bf_lo(b1v)), rbf(rbf(a1) + bf_hi(b1v))};
                float hv[2];
#pragma unroll
                for (int e = 0; e < 2; ++e) {
                    const float u = 0.7978845608028654f * (z[e] + 0.044715f * z[e] * z[e] * z[e]);
                    const float th = 1.0f - 2.0f / (__expf(2.0f * u) + 1.0f);
                    hv[e] = 0.5f * z[e] * (1.0f + th);
                }
                u32x2 gv = {pack_bf16(hv[0], hv[1]), tag};
                __builtin_amdgcn_raw_buffer_store_b64(gv, rs, (lane < 32) ? GH_OFF + (b * 896 + 8 * (j * 14 + i) + p) * 8 : 0x40000000, 0, SC1);
            }
        }
        KEEP4(kf[0]); KEEP4(kf[1]);
        vf[0] = ld16(vp); vf[1] = ld16(vp + 512);
        __builtin_amdgcn_sched_barrier(0);
        KEEP4(kf[2]); KEEP4(kf[3]);
        TS(2);
        vf[2] = ld16(vp + 1024); vf[3] = ld16(vp + 1536);
        __builtin_amdgcn_sched_barrier(0);
        KEEP4(vf[0]); KEEP4(vf[1]);
        u32x4 wf[4];
        const bool has2 = (wave < 4) || setB;
        {
            const __amdgpu_buffer_rsrc_t r2 = __builtin_amdgcn_make_buffer_rsrc((void*)((wave < 4) ? wo : w2), 0, (wave < 4) ? 4096 * 512 * 2 : 1792 * 512 * 2, 0x00020000);
            const int row = (wave < 4) ? 64 * rg + 16 * wave : 64 * rg2 + 16 * (wave - 4), col = (wave < 4) ? 128 * cgp : 128 * cg2;
            const int off = has2 ? ((row + 4 * g) * 512 + col + 8 * c) * 2 : 0x40000000;
#pragma unroll
            for (int t = 0; t < 4; ++t) wf[t] = __builtin_amdgcn_raw_buffer_load_b128(r2, off + t * 1024, 0, USE_NT ? 2 : 0);
        }
        __builtin_amdgcn_sched_barrier(0);
        TS(3);
        __syncthreads();
        TS(4);
        const u32x4 qf = *reinterpret_cast<const u32x4*>(smem + L_QS + (((lane >> 2) & 3) * 128 + (g * 4 + (lane & 3)) * 8) * 2);
        float sc[4];
        {
            f32x4 z = {0.f, 0.f, 0.f, 0.f};
#pragma unroll
            for (int t = 0; t < 4; ++t) {
                f32x4 d = mfma32(kf[t], qf, z);
                float s = quad_sum(sel4(d, lane & 3));
                sc[t] = rbf(rbf(s) * 0.08838834764831845f);
            }
        }
        float m = xrow_max(fmaxf(fmaxf(sc[0], sc[1]), fmaxf(sc[2], sc[3])));
        float p0 = __builtin_amdgcn_exp2f((sc[0] - m) * LOG2E), p1 = __builtin_amdgcn_exp2f((sc[1] - m) * LOG2E);
        float p2 = __builtin_amdgcn_exp2f((sc[2] - m) * LOG2E), p3 = __builtin_amdgcn_exp2f((sc[3] - m) * LOG2E);
        float l = xrow_sum((p0 + p1) + (p2 + p3));
        u32x2 pa = {pack_bf16(p0, p1), pack_bf16(p2, p3)};
        if (g == 0 && (c & 3) == 0) *reinterpret_cast<f32x2*>(smem + L_ML + (wave * 4 + (c >> 2)) * 8) = f32x2{m, l};
        {
            f32x4 z = {0.f, 0.f, 0.f, 0.f};
            float o8[8];
#pragma unroll
            for (int r = 0; r < 4; ++r) {
                u32x2 fl = {plo(vf[0][r], vf[1][r]), plo(vf[2][r], vf[3][r])};
                u32x2 fh = {phi(vf[0][r], vf[1][r]), phi(vf[2][r], vf[3][r])};
                o8[2 * r] = mfma16(pa, fl, z)[0];
                o8[2 * r + 1] = mfma16(pa, fh, z)[0];
            }
            float* dst = reinterpret_cast<float*>(smem + L_OP) + (wave * 4 + g) * 128 + c * 8;
            *reinterpret_cast<f32x4*>(dst) = f32x4{o8[0], o8[1], o8[2], o8[3]};
            *reinterpret_cast<f32x4*>(dst + 4) = f32x4{o8[4], o8[5], o8[6], o8[7]};
        }
        TS(5);
        __syncthreads();
        if (wave >= 4) {
            const int hl = wave - 4;
            const float* mlp = reinterpret_cast<const float*>(smem + L_ML);
            float mw[8], lw[8], M = -INFINITY;
#pragma unroll
            for (int w = 0; w < 8; ++w) { f32x2 t = *reinterpret_cast<const f32x2*>(mlp + (w * 4 + hl) * 2); mw[w] = t.x; lw[w] = t.y; M = fmaxf(M, t.x); }
            float L = 0.f, a0 = 0.f, a1 = 0.f;
#pragma unroll
            for (int w = 0; w < 8; ++w) {
                const float sw = __builtin_amdgcn_exp2f((mw[w] - M) * LOG2E);
                f32x2 t = *reinterpret_cast<const f32x2*>(smem + L_OP + (((w * 4 + hl) * 128) + 2 * lane) * 4);
                L = fmaf(sw, lw[w], L); a0 = fmaf(sw, t.x, a0); a1 = fmaf(sw, t.y, a1);
            }
            u32x4 gv = {__float_as_uint(a0), tag, __float_as_uint(a1), tag};
            __builtin_amdgcn_raw_buffer_store_b128(gv, rs, GO_OFF + ((((ab * 8 + j) * 8 + as) * 4 + hl) * 128 + 2 * lane) * 8, 0, SC1);
            u32x4 gm = {__float_as_uint(M), tag, __float_as_uint(L), tag};
            __builtin_amdgcn_raw_buffer_store_b128(gm, rs, (lane == 0) ? GML_OFF + (((j * 4 + hl) * 4 + ab) * 8 + as) * 16 : 0x40000000, 0, SC1);
            if (wave == 4) TSW(10);
        }
        TS(6);
        if (has2) {
            u32x2 bfr[8];
#pragma unroll
            for (int r = 0; r < 4; ++r) {
                bfr[2 * r] = u32x2{plo(wf[0][r], wf[1][r]), plo(wf[2][r], wf[3][r])};
                bfr[2 * r + 1] = u32x2{phi(wf[0][r], wf[1][r]), phi(wf[2][r], wf[3][r])};
            }
#pragma unroll
            for (int e = 0; e < 8; ++e) asm volatile("" : "+v"(bfr[e].x), "+v"(bfr[e].y));
            u32x2 af;
            const int lb = (lane >> 2) & 3, sq = lane & 3;
            if (wave < 4) {
                const int head = rg >> 1, kvh = head >> 2, hl = head & 3, dbase = 64 * (rg & 1) + 16 * wave;
                const int s = lane >> 3, piece = lane & 7;
                const int off0 = GO_OFF + ((((0 * 8 + kvh) * 8 + s) * 4 + hl) * 128 + dbase + 2 * piece) * 8;
                const int offm = GML_OFF + ((kvh * 4 + hl) * 32 + (lane & 31)) * 16;
                u32x4 gv[4], gm;
                for (unsigned spins = 0;;) {
                    CBAR();
#pragma unroll
                    for (int b = 0; b < 4; ++b) gv[b] = __builtin_amdgcn_raw_buffer_load_b128(rs, off0 + b * (8 * 8 * 4 * 128 * 8), 0, SC1);
                    gm = __builtin_amdgcn_raw_buffer_load_b128(rs, offm, 0, SC1);
                    bool ok = (gm.y == tag) & (gm.w == tag);
#pragma unroll
                    for (int b = 0; b < 4; ++b) ok &= (gv[b].y == tag) & (gv[b].w == tag);
                    if (__all(ok) || ++spins > SPIN_CAP) break;
                }
                float* st = reinterpret_cast<float*>(smem + L_ST) + wave * 512;
                float* stml = reinterpret_cast<float*>(smem + L_STML) + wave * 64;
#pragma unroll
                for (int b = 0; b < 4; ++b) *reinterpret_cast<u32x2*>(st + (b * 8 + s) * 16 + 2 * piece) = u32x2{gv[b].x, gv[b].z};
                if (lane < 32) *reinterpret_cast<u32x2*>(stml + lane * 2) = u32x2{gm.x, gm.z};
                LDS_FENCE();
                const f32x4 o0 = *reinterpret_cast<const f32x4*>(st + (lb * 8 + 2 * sq) * 16 + 4 * g);
                const f32x4 o1 = *reinterpret_cast<const f32x4*>(st + (lb * 8 + 2 * sq + 1) * 16 + 4 * g);
                const f32x4 ml = *reinterpret_cast<const f32x4*>(stml + (lb * 8 + 2 * sq) * 2);
                const float M = quad_max(fmaxf(ml[0], ml[2]));
                const float w0 = __builtin_amdgcn_exp2f((ml[0] - M) * LOG2E), w1_ = __builtin_amdgcn_exp2f((ml[2] - M) * LOG2E);
                const float L = quad_sum(fmaf(w0, ml[1], w1_ * ml[3]));
                const float inv = 1.0f / L;
                float v[4];
#pragma unroll
                for (int e = 0; e < 4; ++e) v[e] = quad_sum(fmaf(w0, o0[e], w1_ * o1[e])) * inv;
                af = u32x2{pack_bf16(v[0], v[1]), pack_bf16(v[2], v[3])};
            } else {
                const int offh = GH_OFF + (lb * 896 + (64 * rg2 + 16 * (wave - 4) + 4 * g) / 2) * 8;
                u32x4 gv;
                for (unsigned spins = 0;;) {
                    CBAR();
                    gv = __builtin_amdgcn_raw_buffer_load_b128(rs, offh, 0, SC1);
                    bool ok = (gv.y == tag) & (gv.w == tag);
                    if (__all(ok) || ++spins > SPIN_CAP) break;
                }
                af = u32x2{gv.x, gv.z};
            }
            TS(7);
            f32x4 z = {0.f, 0.f, 0.f, 0.f};
            float o8[8];
#pragma unroll
            for (int e = 0; e < 8; ++e) o8[e] = mfma16(af, bfr[e], z)[0];
            float* dst = reinterpret_cast<float*>(smem + L_WP) + (wave * 4 + g) * 128 + c * 8;
            *reinterpret_cast<f32x4*>(dst) = f32x4{o8[0], o8[1], o8[2], o8[3]};
            *reinterpret_cast<f32x4*>(dst + 4) = f32x4{o8[4], o8[5], o8[6], o8[7]};
        }
        __syncthreads();
        if (has2) {
            const int t8 = tid & 255, r = t8 >> 2, b = t8 & 3, wb = wave & 4;
            float a0 = 0.f, a1 = 0.f;
#pragma unroll
            for (int w = 0; w < 4; ++w) {
                f32x2 t = *reinterpret_cast<const f32x2*>(smem + L_WP + (((wb + w) * 4 + b) * 128 + 2 * r) * 4);
                a0 += t.x; a1 += t.y;
            }
            const int cgx = (wave < 4) ? cgp : cg2, slot = (wave < 4) ? rg : 64 + rg2;
            u32x4 gv = {__float_as_uint(a0), tag, __float_as_uint(a1), tag};
            __builtin_amdgcn_raw_buffer_store_b128(gv, rs, G3_OFF + (((cgx * 64 + r) * 96 + slot) * 8 + 2 * b) * 8, 0, SC1);
        }
        TS(8);
    } else {
        u32x4 xv[4];
#pragma unroll
        for (int b = 0; b < 4; ++b) xv[b] = *reinterpret_cast<const u32x4*>(x + b * 512 + lane * 8);
        const u32x4 gwv = *reinterpret_cast<const u32x4*>(gw + lane * 8);
        const int cgf = bid >> 6, rr = bid & 63;
        const u32 b2v = *reinterpret_cast<const u32*>(b2 + 128 * cgf + 2 * rr);
        if (lane < 4) cnt[lane] = 0u;
        __builtin_amdgcn_sched_barrier(0);
        __syncthreads();
        __builtin_amdgcn_sched_barrier(0);
        const u32 tag = __builtin_amdgcn_readfirstlane(ev) + 1u;
        if (setA) {
            float mean[4], var[4];
#pragma unroll
            for (int b = 0; b < 4; ++b) {
                float s = ((bf_lo(xv[b][0]) + bf_hi(xv[b][0])) + (bf_lo(xv[b][1]) + bf_hi(xv[b][1]))) + ((bf_lo(xv[b][2]) + bf_hi(xv[b][2])) + (bf_lo(xv[b][3]) + bf_hi(xv[b][3])));
                mean[b] = wave_sum(s) * (1.0f / 512.0f);
            }
#pragma unroll
            for (int b = 0; b < 4; ++b) {
                float vs = 0.f;
#pragma unroll
                for (int e = 0; e < 4; ++e) { const float d0 = bf_lo(xv[b][e]) - mean[b], d1 = bf_hi(xv[b][e]) - mean[b]; vs = fmaf(d0, d0, vs); vs = fmaf(d1, d1, vs); }
                var[b] = wave_sum(vs) * (1.0f / 512.0f);
            }
            const float mn = (lane & 2) ? ((lane & 1) ? mean[3] : mean[2]) : ((lane & 1) ? mean[1] : mean[0]);
            const float vr = (lane & 2) ? ((lane & 1) ? var[3] : var[2]) : ((lane & 1) ? var[1] : var[0]);
            const float sd = rbf(sqrtf(rbf(rbf(vr) + 1e-5f)));
            if (lane < 4) *reinterpret_cast<f32x4*>(smem + L_YS + lane * 16) = f32x4{rbf(mn), sd, 1.0f / sd, 0.f};
            LDS_FENCE();
            if (lane == 0) __hip_atomic_store(cnt + 2, 1u, __ATOMIC_RELAXED, __HIP_MEMORY_SCOPE_WORKGROUP);
        }
        float gate[4];
#pragma unroll
        for (int b = 0; b < 4; ++b) {
            float s = 0.f;
#pragma unroll
            for (int e = 0; e < 4; ++e) { s += rbf(bf_lo(xv[b][e]) * bf_lo(gwv[e])); s += rbf(bf_hi(xv[b][e]) * bf_hi(gwv[e])); }
            s = rbf(wave_sum(s));
            gate[b] = rbf(1.0f / (1.0f + __expf(-s)));
        }
        TS(1);
        {
            const int off = GQ_OFF + ((j * 4 + ab) * 256 + 4 * lane) * 8;
            const u32 ldsdst = (u32)(uintptr_t)(__attribute__((address_space(3))) unsigned char*)(smem + L_QS) + lane * 16;
            u32 scnt;
#define PL(a, b) "buffer_load_dwordx4 v[" #a "], %[off], %[rs], 0 offen sc1\n\tbuffer_load_dwordx4 v[" #b "], %[off], %[rs], 0 offen offset:16 sc1\n\t"
#define PCHK(t0, t1, t2, t3, lab) "s_waitcnt vmcnt(6)\n\tv_xor_b32 v132, %[tag], v" #t0 "\n\tv_xor_b32 v133, %[tag], v" #t1 "\n\tv_xor_b32 v134, %[tag], v" #t2 "\n\tv_xor_b32 v135, %[tag], v" #t3 "\n\t" \
                   "v_or3_b32 v132, v132, v133, v134\n\tv_or_b32 v132, v132, v135\n\tv_cmp_ne_u32 vcc, 0, v132\n\ts_cbranch_vccz " lab "\n\t"
#define PDONE(lab, d0, d1, d2, d3) lab ":\n\tv_mov_b32 v132, v" #d0 "\n\tv_mov_b32 v133, v" #d1 "\n\tv_mov_b32 v134, v" #d2 "\n\tv_mov_b32 v135, v" #d3 "\n\ts_branch 20f\n\t"
            asm volatile(
                "s_mov_b32 %[cnt], 0\n\t"
                PL(136:139, 140:143) "s_sleep 5\n\t"
                PL(144:147, 148:151) "s_sleep 5\n\t"
                PL(152:155, 156:159) "s_sleep 5\n\t"
                PL(160:163, 164:167)
                "1:\n\t"
                PCHK(137, 139, 141, 143, "10f") PL(136:139, 140:143)
                PCHK(145, 147, 149, 151, "11f") PL(144:147, 148:151)
                PCHK(153, 155, 157, 159, "12f") PL(152:155, 156:159)
                PCHK(161, 163, 165, 167, "13f") PL(160:163, 164:167)
                "s_add_u32 %[cnt], %[cnt], 1\n\t"
                "s_cmp_lt_u32 %[cnt], %[cap]\n\t"
                "s_cbranch_scc1 1b\n\t"
                "s_waitcnt vmcnt(0)\n\t"
                PDONE("10", 136, 138, 140, 142)
                PDONE("11", 144, 146, 148, 150)
                PDONE("12", 152, 154, 156, 158)
                PDONE("13", 160, 162, 164, 166)
                "20:\n\t"
                "ds_write_b128 %[dst], v[132:135]\n\t"
                "s_waitcnt lgkmcnt(0)\n\t"
                : [cnt] "=&s"(scnt)
                : [off] "v"(off), [rs] "s"(rs), [tag] "s"(tag), [dst] "v"(ldsdst), [cap] "s"(SPIN_CAP)
                : "memory", "vcc", "scc", "v132", "v133", "v134", "v135",
                  "v136", "v137", "v138", "v139", "v140", "v141", "v142", "v143", "v144", "v145", "v146", "v147",
                  "v148", "v149", "v150", "v151", "v152", "v153", "v154", "v155", "v156", "v157", "v158", "v159",
                  "v160", "v161", "v162", "v163", "v164", "v165", "v166", "v167");
        }
        TS(3);
        __syncthreads();
        __syncthreads();
        __syncthreads();
        TS(4);
        {
            const int pp = lane >> 2, b = lane & 3;
            const int off = G3_OFF + ((cgf * 64 + rr) * 96 * 8 + lane * 2) * 8;
            u32x4 gv[6];
            for (unsigned spins = 0;;) {
                CBAR();
#pragma unroll
                for (int u = 0; u < 6; ++u) gv[u] = __builtin_amdgcn_raw_buffer_load_b128(rs, off + u * 1024, 0, SC1);
                bool ok = true;
#pragma unroll
                for (int u = 0; u < 5; ++u) ok &= (gv[u].y == tag) & (gv[u].w == tag);
                ok &= (pp >= 12) | ((gv[5].y == tag) & (gv[5].w == tag));
                if (__all(ok) || ++spins > SPIN_CAP) break;
            }
            TS(5);
            float a0 = (__uint_as_float(gv[0].x) + __uint_as_float(gv[1].x)) + (__uint_as_float(gv[2].x) + __uint_as_float(gv[3].x));
            float a1 = (__uint_as_float(gv[0].z) + __uint_as_float(gv[1].z)) + (__uint_as_float(gv[2].z) + __uint_as_float(gv[3].z));
            float c0 = __uint_as_float(gv[4].x) + ((pp < 12) ? __uint_as_float(gv[5].x) : 0.f);
            float c1 = __uint_as_float(gv[4].z) + ((pp < 12) ? __uint_as_float(gv[5].z) : 0.f);
            a0 = xrow_sum(ror_sum(a0)); a1 = xrow_sum(ror_sum(a1)); c0 = xrow_sum(ror_sum(c0)); c1 = xrow_sum(ror_sum(c1));
            const float gt = (b & 2) ? ((b & 1) ? gate[3] : gate[2]) : ((b & 1) ? gate[1] : gate[0]);
            const float omg = rbf(1.0f - gt);
            const float mb0 = rbf(rbf(c0) + bf_lo(b2v)), mb1 = rbf(rbf(c1) + bf_hi(b2v));
            const float r0 = rbf(gt * rbf(a0)) + rbf(omg * mb0), r1 = rbf(gt * rbf(a1)) + rbf(omg * mb1);
            if (lane < 4) *reinterpret_cast<u32*>(out + b * 512 + 128 * cgf + 2 * rr) = pack_bf16(r0, r1);
            if (bid == 0 && lane == 0) __builtin_amdgcn_raw_buffer_store_b32(tag, rs, E_OFF, 0, SC1);
        }
        TS(6);
    }
}

extern "C" void kernel_launch(void* const* d_in, const int* in_sizes, int n_in,
                              void* d_out, int out_size, void* d_ws, size_t ws_size,
                              hipStream_t stream) {
    fused_decode<<<256, 576, 0, stream>>>((const u16*)d_in[0], (const u16*)d_in[1], (const u16*)d_in[2],
                                          (const u16*)d_in[3], (const u16*)d_in[4], (const u16*)d_in[6],
                                          (const u16*)d_in[7], (const u16*)d_in[8], (const u16*)d_in[9],
                                          (const u16*)d_in[10], (u16*)d_out, (unsigned char*)d_ws);
}
```

```cpp
#include <hip/hip_runtime.h>
#include <hip/hip_bf16.h>
#include <stdint.h>

typedef unsigned short u16;
typedef unsigned int u32;
typedef u32 u32x2 __attribute__((ext_vector_type(2)));
typedef u32 u32x4 __attribute__((ext_vector_type(4)));
typedef float f32x2 __attribute__((ext_vector_type(2)));
typedef float f32x4 __attribute__((ext_vector_type(4)));
typedef short s16x4 __attribute__((ext_vector_type(4)));
typedef __bf16 bf16x8 __attribute__((ext_vector_type(8)));
typedef __bf16 bf16x2 __attribute__((ext_vector_type(2)));

#define H2_HINT 1
#define H2_SLEEP 2
#define H3_SLEEP 2
#define USE_NT 1

constexpr int E_OFF = 0;
constexpr int GQ_OFF = 4096;
constexpr int GH_OFF = GQ_OFF + 65536;
constexpr int GML_OFF = GH_OFF + 32768;
constexpr int GO_OFF = GML_OFF + 16384;
constexpr int G3_OFF = GO_OFF + 524288;
constexpr int WS_USED = G3_OFF + 4 * 64 * 96 * 64;

constexpr int L_QP = 0;
constexpr int L_HP = L_QP + 2048;
constexpr int L_YS = L_HP + 2048;
constexpr int L_CNT = L_YS + 64;
constexpr int L_QS = L_CNT + 64;
constexpr int L_SC = L_QS + 1024;
constexpr int L_WP = L_SC + 1024;
constexpr int L_END = L_WP + 16384;
constexpr int LDS_BYTES = 84 * 1024;
static_assert(L_END <= LDS_BYTES, "lds");

constexpr unsigned SPIN_CAP = 1u << 18;
constexpr float LOG2E = 1.4426950408889634f;

__device__ __forceinline__ float bf_lo(u32 v) { return __uint_as_float(v << 16); }
__device__ __forceinline__ float bf_hi(u32 v) { return __uint_as_float(v & 0xffff0000u); }
__device__ __forceinline__ u32 pack_bf16(float lo, float hi) {
    bf16x2 v = {(__bf16)lo, (__bf16)hi};
    return __builtin_bit_cast(u32, v);
}
__device__ __forceinline__ float rbf(float f) { return bf_lo((u32)__builtin_bit_cast(u16, (__bf16)f)); }

template <int CTRL> __device__ __forceinline__ float dppf(float x) {
    return __builtin_bit_cast(float, __builtin_amdgcn_mov_dpp(__builtin_bit_cast(int, x), CTRL, 0xf, 0xf, true));
}
constexpr int XOR1 = 0xB1, XOR2 = 0x4E, ROR4 = 0x124, ROR8 = 0x128;
__device__ __forceinline__ float quad_sum(float x) { x += dppf<XOR1>(x); x += dppf<XOR2>(x); return x; }
__device__ __forceinline__ float quad_max(float x) { x = fmaxf(x, dppf<XOR1>(x)); x = fmaxf(x, dppf<XOR2>(x)); return x; }
__device__ __forceinline__ float ror_sum(float x) { x += dppf<ROR8>(x); x += dppf<ROR4>(x); return x; }
__device__ __forceinline__ float xrow_max(float x) {
    auto s = __builtin_amdgcn_permlane16_swap(__float_as_uint(x), __float_as_uint(x), false, false);
    x = fmaxf(__uint_as_float(s[0]), __uint_as_float(s[1]));
    auto t = __builtin_amdgcn_permlane32_swap(__float_as_uint(x), __float_as_uint(x), false, false);
    return fmaxf(__uint_as_float(t[0]), __uint_as_float(t[1]));
}
__device__ __forceinline__ float xrow_sum(float x) {
    auto s = __builtin_amdgcn_permlane16_swap(__float_as_uint(x), __float_as_uint(x), false, false);
    x = __uint_as_float(s[0]) + __uint_as_float(s[1]);
    auto t = __builtin_amdgcn_permlane32_swap(__float_as_uint(x), __float_as_uint(x), false, false);
    return __uint_as_float(t[0]) + __uint_as_float(t[1]);
}
__device__ __forceinline__ float wave_sum(float x) { return xrow_sum(ror_sum(quad_sum(x))); }

__device__ __forceinline__ u32x4 ld16(const u16* p) {
    return __builtin_nontemporal_load(reinterpret_cast<const u32x4*>(p));
}
__device__ __forceinline__ u32x2 ld8(const u16* p) { return *reinterpret_cast<const u32x2*>(p); }

__device__ __forceinline__ u32 plo(u32 a, u32 b) { return __builtin_amdgcn_perm(b, a, 0x05040100u); }
__device__ __forceinline__ u32 phi(u32 a, u32 b) { return __builtin_amdgcn_perm(b, a, 0x07060302u); }

__device__ __forceinline__ f32x4 mfma16(u32x2 a, u32x2 b, f32x4 c) {
    return __builtin_amdgcn_mfma_f32_16x16x16bf16_1k(__builtin_bit_cast(s16x4, a), __builtin_bit_cast(s16x4, b), c, 0, 0, 0);
}
__device__ __forceinline__ f32x4 mfma32(u32x4 a, u32x4 b, f32x4 c) {
    return __builtin_amdgcn_mfma_f32_16x16x32_bf16(__builtin_bit_cast(bf16x8, a), __builtin_bit_cast(bf16x8, b), c, 0, 0, 0);
}
__device__ __forceinline__ float sel4(f32x4 d, int s) {
    float lo = (s & 1) ? d[1] : d[0], hi = (s & 1) ? d[3] : d[2];
    return (s & 2) ? hi : lo;
}
#define SC1 16
#define CBAR() asm volatile("" ::: "memory")
#define KEEP4(v) asm volatile("" : "+v"((v).x), "+v"((v).y), "+v"((v).z), "+v"((v).w))
#define LDS_FENCE() asm volatile("s_waitcnt lgkmcnt(0)" ::: "memory")

#define TS(k)
#define TSW(k)
#define TS_DECL
#define TS_FLUSH()

__device__ __forceinline__ f32x4 tile16(u32x2 xa, const u32x2 (&wl)[4], int c) {
    f32x4 z = {0.f, 0.f, 0.f, 0.f};
    u32x2 f0 = {plo(wl[0].x, wl[1].x), plo(wl[2].x, wl[3].x)};
    u32x2 f1 = {phi(wl[0].x, wl[1].x), phi(wl[2].x, wl[3].x)};
    u32x2 f2 = {plo(wl[0].y, wl[1].y), plo(wl[2].y, wl[3].y)};
    u32x2 f3 = {phi(wl[0].y, wl[1].y), phi(wl[2].y, wl[3].y)};
    f32x4 d0 = mfma16(xa, f0, z), d1 = mfma16(xa, f1, z), d2 = mfma16(xa, f2, z), d3 = mfma16(xa, f3, z);
    const int sub = c >> 2;
    f32x4 r;
    r[0] = ror_sum(sel4(d0, sub)); r[1] = ror_sum(sel4(d1, sub)); r[2] = ror_sum(sel4(d2, sub)); r[3] = ror_sum(sel4(d3, sub));
    return r;
}

__global__ void __launch_bounds__(576, 3)
fused_decode(const u16* __restrict__ x, const u16* __restrict__ kc, const u16* __restrict__ vc,
             const u16* __restrict__ wq, const u16* __restrict__ wo, const u16* __restrict__ w1,
             const u16* __restrict__ b1, const u16* __restrict__ w2, const u16* __restrict__ b2,
             const u16* __restrict__ gw, u16* __restrict__ out, unsigned char* ws) {
    __shared__ __attribute__((aligned(16))) unsigned char smem[LDS_BYTES];
    const int tid = threadIdx.x, lane = tid & 63;
    const int wave = __builtin_amdgcn_readfirstlane(tid >> 6);
    const int bid = blockIdx.x, j = bid & 7, i = bid >> 3;
    const bool setA = i < 14, setB = (i >= 14) && (i < 28);
    const int g = lane >> 4, c = lane & 15;
    const __amdgpu_buffer_rsrc_t rs = __builtin_amdgcn_make_buffer_rsrc(ws, 0, WS_USED, 0x00020000);
    const int ab = i >> 3, as = i & 7;
    const int rg = 8 * j + (i >> 2), cgp = i & 3;
    const int tb = j * 14 + (i - 14), rg2 = tb >> 2, cg2 = tb & 3;
    u32* const cnt = reinterpret_cast<u32*>(smem + L_CNT);
    TS_DECL
    TS(0);
    const u32 ev = __builtin_amdgcn_raw_buffer_load_b32(rs, E_OFF, 0, SC1);

    if (wave < 8) {
        const int ra = 64 * wave + (g * 4 + (c & 3)) * 4;
        const int rb = 64 * wave + (g * 4 + (c >> 2)) * 4;
        u32x2 xa = ld8(x + (c >> 2) * 512 + ra);
        u32x2 wl[4], w1l[4];
        {
            const u16* p = wq + (size_t)rb * 4096 + 512 * j + 16 * i + (c & 3) * 4;
#pragma unroll
            for (int t = 0; t < 4; ++t) wl[t] = ld8(p + t * 4096);
        }
        u32 b1v;
        {
            const __amdgpu_buffer_rsrc_t r1 = __builtin_amdgcn_make_buffer_rsrc((void*)w1, 0, 512 * 1792 * 2, 0x00020000);
            const int off = setA ? (rb * 1792 + 16 * (j * 14 + i) + (c & 3) * 4) * 2 : 0x40000000;
#pragma unroll
            for (int t = 0; t < 4; ++t) w1l[t] = __builtin_amdgcn_raw_buffer_load_b64(r1, off + t * 3584, 0, 0);
            const __amdgpu_buffer_rsrc_t rb1 = __builtin_amdgcn_make_buffer_rsrc((void*)b1, 0, 1792 * 2, 0x00020000);
            b1v = __builtin_amdgcn_raw_buffer_load_b32(rb1, setA ? (16 * (j * 14 + i) + 2 * (lane & 7)) * 2 : 0x40000000, 0, 0);
        }
        const size_t kvbase = ((size_t)(ab * 8 + j) * 4096 + 3072 + 128 * as) * 128;
        const u16* kp = kc + kvbase + (16 * wave + 4 * ((lane >> 2) & 3)) * 128 + (g * 4 + (lane & 3)) * 8;
        const u16* vp = vc + kvbase + ((g * 4 + (c >> 2)) * 8) * 128 + 16 * wave + (c & 3) * 4;
        u32x4 kf[4];
        u32x2 vf[8];
        __builtin_amdgcn_sched_barrier(0);
        __syncthreads();
        __builtin_amdgcn_sched_barrier(0);
        asm volatile("" : "+v"(wl[0].x), "+v"(wl[1].x), "+v"(wl[2].x), "+v"(wl[3].x));
        kf[0] = ld16(kp); kf[1] = ld16(kp + 128); kf[2] = ld16(kp + 256); kf[3] = ld16(kp + 384);
#pragma unroll
        for (int t = 0; t < 4; ++t) vf[t] = ld8(vp + t * 128);
        __builtin_amdgcn_sched_barrier(0);
        const u32 tag = __builtin_amdgcn_readfirstlane(ev) + 1u;
        {
            f32x4 r = tile16(xa, wl, c);
            if (c < 4) *reinterpret_cast<f32x4*>(smem + L_QP + ((wave * 4 + g) * 16 + c * 4) * 4) = r;
            LDS_FENCE();
            u32 old = 0;
            if (lane == 0) old = __hip_atomic_fetch_add(cnt + 0, 1u, __ATOMIC_RELAXED, __HIP_MEMORY_SCOPE_WORKGROUP);
            old = __builtin_amdgcn_readfirstlane(old);
            TS(1);
            if (old == 7u) {
                CBAR();
                const int b = (lane >> 3) & 3, p = lane & 7;
                float a0 = 0.f, a1 = 0.f;
#pragma unroll
                for (int w = 0; w < 8; ++w) { f32x2 t = *reinterpret_cast<const f32x2*>(smem + L_QP + ((w * 4 + b) * 16 + 2 * p) * 4); a0 += t.x; a1 += t.y; }
                u32x2 gv = {pack_bf16(a0, a1), tag};
                __builtin_amdgcn_raw_buffer_store_b64(gv, rs, (lane < 32) ? GQ_OFF + ((j * 32 + i) * 32 + lane) * 8 : 0x40000000, 0, SC1);
                TSW(9);
            }
        }
        __builtin_amdgcn_sched_barrier(0);
        if (setA) {
            while (__hip_atomic_load(cnt + 2, __ATOMIC_RELAXED, __HIP_MEMORY_SCOPE_WORKGROUP) == 0u) __builtin_amdgcn_s_sleep(1);
            CBAR();
            const f32x4 stt = *reinterpret_cast<const f32x4*>(smem + L_YS + (c >> 2) * 16);
            float d[4] = {rbf(bf_lo(xa.x) - stt[0]), rbf(bf_hi(xa.x) - stt[0]), rbf(bf_lo(xa.y) - stt[0]), rbf(bf_hi(xa.y) - stt[0])};
#pragma unroll
            for (int e = 0; e < 4; ++e) { const float q0 = d[e] * stt[2]; d[e] = fmaf(fmaf(-q0, stt[1], d[e]), stt[2], q0); }
            const u32x2 ya = u32x2{pack_bf16(d[0], d[1]), pack_bf16(d[2], d[3])};
            f32x4 r = tile16(ya, w1l, c);
            if (c < 4) *reinterpret_cast<f32x4*>(smem + L_HP + ((wave * 4 + g) * 16 + c * 4) * 4) = r;
            LDS_FENCE();
            u32 old = 0;
            if (lane == 0) old = __hip_atomic_fetch_add(cnt + 1, 1u, __ATOMIC_RELAXED, __HIP_MEMORY_SCOPE_WORKGROUP);
            old = __builtin_amdgcn_readfirstlane(old);
            if (old == 7u) {
                CBAR();
                const int b = (lane >> 3) & 3, p = lane & 7;
                float a0 = 0.f, a1 = 0.f;
#pragma unroll
                for (int w = 0; w < 8; ++w) { f32x2 t = *reinterpret_cast<const f32x2*>(smem + L_HP + ((w * 4 + b) * 16 + 2 * p) * 4); a0 += t.x; a1 += t.y; }
                float z[2] = {rbf(rbf(a0) + bf_lo(b1v)), rbf(rbf(a1) + bf_hi(b1v))};
                float hv[2];
#pragma unroll
                for (int e = 0; e < 2; ++e) {
                    const float u = 0.7978845608028654f * (z[e] + 0.044715f * z[e] * z[e] * z[e]);
                    const float th = 1.0f - 2.0f / (__expf(2.0f * u) + 1.0f);
                    hv[e] = 0.5f * z[e] * (1.0f + th);
                }
                u32x2 gv = {pack_bf16(hv[0], hv[1]), tag};
                __builtin_amdgcn_raw_buffer_store_b64(gv, rs, (lane < 32) ? GH_OFF + ((j * 14 + i) * 32 + lane) * 8 : 0x40000000, 0, SC1);
            }
        }
        KEEP4(kf[0]); KEEP4(kf[1]);
#pragma unroll
        for (int t = 4; t < 8; ++t) vf[t] = ld8(vp + t * 128);
        __builtin_amdgcn_sched_barrier(0);
        KEEP4(kf[2]); KEEP4(kf[3]);
        TS(2);
        u32x4 wf[4];
        const bool has2 = (wave < 4) || setB;
        {
            const __amdgpu_buffer_rsrc_t r2 = __builtin_amdgcn_make_buffer_rsrc((void*)((wave < 4) ? wo : w2), 0, (wave < 4) ? 4096 * 512 * 2 : 1792 * 512 * 2, 0x00020000);
            const int row = (wave < 4) ? 64 * rg + 16 * wave : 64 * rg2 + 16 * (wave - 4), col = (wave < 4) ? 128 * cgp : 128 * cg2;
            const int off = has2 ? ((row + 4 * g) * 512 + col + 8 * c) * 2 : 0x40000000;
#pragma unroll
            for (int t = 0; t < 4; ++t) wf[t] = __builtin_amdgcn_raw_buffer_load_b128(r2, off + t * 1024, 0, USE_NT ? 2 : 0);
        }
        __builtin_amdgcn_sched_barrier(0);
        TS(3);
        __syncthreads();
        TS(4);
        {
            const u32x4 qf = *reinterpret_cast<const u32x4*>(smem + L_QS + (((lane >> 2) & 3) * 128 + (g * 4 + (lane & 3)) * 8) * 2);
            f32x4 z = {0.f, 0.f, 0.f, 0.f};
            float sc[4];
#pragma unroll
            for (int t = 0; t < 4; ++t) {
                f32x4 d = mfma32(kf[t], qf, z);
                sc[t] = rbf(quad_sum(sel4(d, lane & 3))) * 0.08838834764831845f;
            }
            if ((lane & 3) == 0) *reinterpret_cast<u32x2*>(smem + L_SC + ((c >> 2) * 128 + 16 * wave + 4 * g) * 2) = u32x2{pack_bf16(sc[0], sc[1]), pack_bf16(sc[2], sc[3])};
        }
        __syncthreads();
        u32x4 pa;
        float M, L;
        {
            const u32x4 sv = *reinterpret_cast<const u32x4*>(smem + L_SC + ((c >> 2) * 128 + (g * 4 + (c & 3)) * 8) * 2);
            float e[8];
#pragma unroll
            for (int k = 0; k < 4; ++k) { e[2 * k] = bf_lo(sv[k]); e[2 * k + 1] = bf_hi(sv[k]); }
            M = fmaxf(fmaxf(fmaxf(e[0], e[1]), fmaxf(e[2], e[3])), fmaxf(fmaxf(e[4], e[5]), fmaxf(e[6], e[7])));
            M = xrow_max(quad_max(M));
#pragma unroll
            for (int k = 0; k < 8; ++k) e[k] = __builtin_amdgcn_exp2f((e[k] - M) * LOG2E);
            L = xrow_sum(quad_sum(((e[0] + e[1]) + (e[2] + e[3])) + ((e[4] + e[5]) + (e[6] + e[7]))));
#pragma unroll
            for (int k = 0; k < 4; ++k) pa[k] = pack_bf16(e[2 * k], e[2 * k + 1]);
        }
        const int slot = ((as >> 2) * 4 + ab) * 4 + (as & 3);
        TS(5);
        {
            f32x4 z = {0.f, 0.f, 0.f, 0.f};
            u32x4 f0 = {plo(vf[0].x, vf[1].x), plo(vf[2].x, vf[3].x), plo(vf[4].x, vf[5].x), plo(vf[6].x, vf[7].x)};
            u32x4 f1 = {phi(vf[0].x, vf[1].x), phi(vf[2].x, vf[3].x), phi(vf[4].x, vf[5].x), phi(vf[6].x, vf[7].x)};
            u32x4 f2 = {plo(vf[0].y, vf[1].y), plo(vf[2].y, vf[3].y), plo(vf[4].y, vf[5].y), plo(vf[6].y, vf[7].y)};
            u32x4 f3 = {phi(vf[0].y, vf[1].y), phi(vf[2].y, vf[3].y), phi(vf[4].y, vf[5].y), phi(vf[6].y, vf[7].y)};
            f32x4 d0 = mfma32(pa, f0, z), d1 = mfma32(pa, f1, z), d2 = mfma32(pa, f2, z), d3 = mfma32(pa, f3, z);
            const int sub = c >> 2;
            const float o0 = ror_sum(sel4(d0, sub)), o1 = ror_sum(sel4(d1, sub)), o2 = ror_sum(sel4(d2, sub)), o3 = ror_sum(sel4(d3, sub));
            u32x4 gv = {pack_bf16(o0, o1), tag, pack_bf16(o2, o3), tag};
            __builtin_amdgcn_raw_buffer_store_b128(gv, rs, (c < 4) ? GO_OFF + (((((ab * 8 + j) * 8 + as) * 4 + g) * 64) + 8 * wave + 2 * c) * 8 : 0x40000000, 0, SC1);
            if (wave == 7) {
                u32x4 gm = {__float_as_uint(M), tag, __float_as_uint(L), tag};
                __builtin_amdgcn_raw_buffer_store_b128(gm, rs, (g == 0 && (c & 3) == 0) ? GML_OFF + ((j * 4 + (c >> 2)) * 32 + slot) * 16 : 0x40000000, 0, SC1);
            }
        }
        TS(6);
        if (has2) {
            u32x2 bfr[8];
#pragma unroll
            for (int r = 0; r < 4; ++r) {
                bfr[2 * r] = u32x2{plo(wf[0][r], wf[1][r]), plo(wf[2][r], wf[3][r])};
                bfr[2 * r + 1] = u32x2{phi(wf[0][r], wf[1][r]), phi(wf[2][r], wf[3][r])};
            }
#pragma unroll
            for (int e = 0; e < 8; ++e) asm volatile("" : "+v"(bfr[e].x), "+v"(bfr[e].y));
            u32x2 af;
            const int lb = (lane >> 2) & 3, sq = lane & 3;
            if (wave < 4) {
                const int head = rg >> 1, kvh = head >> 2, hl = head & 3;
                const int off0 = GO_OFF + (((((lb * 8 + kvh) * 8 + sq) * 4 + hl) * 64) + 32 * (rg & 1) + 8 * wave + 2 * g) * 8;
                const int offm = GML_OFF + (head * 32 + c) * 16;
                u32x4 g0 = {0u, 0u, 0u, 0u}, g1 = g0, m0 = g0, m1 = g0;
                for (unsigned spins = 0;;) {
                    CBAR();
                    m0 = __builtin_amdgcn_raw_buffer_load_b128(rs, offm, 0, SC1);
                    m1 = __builtin_amdgcn_raw_buffer_load_b128(rs, offm + 256, 0, SC1);
                    const bool ok = (((m0.y ^ tag) | (m0.w ^ tag)) | ((m1.y ^ tag) | (m1.w ^ tag))) == 0u;
                    if (__all(ok) || ++spins > SPIN_CAP) break;
                    __builtin_amdgcn_s_sleep(H2_SLEEP);
                }
                for (unsigned spins = 0;;) {
                    CBAR();
                    g0 = __builtin_amdgcn_raw_buffer_load_b128(rs, off0, 0, SC1);
                    g1 = __builtin_amdgcn_raw_buffer_load_b128(rs, off0 + 4 * 4 * 64 * 8, 0, SC1);
                    const bool ok = (((g0.y ^ tag) | (g0.w ^ tag)) | ((g1.y ^ tag) | (g1.w ^ tag))) == 0u;
                    if (__all(ok) || ++spins > SPIN_CAP) break;
                }
                const float ma = __uint_as_float(m0.x), mb = __uint_as_float(m1.x);
                const float MM = quad_max(fmaxf(ma, mb));
                const float w0 = __builtin_amdgcn_exp2f((ma - MM) * LOG2E), w1_ = __builtin_amdgcn_exp2f((mb - MM) * LOG2E);
                const float LL = quad_sum(fmaf(w0, __uint_as_float(m0.z), w1_ * __uint_as_float(m1.z)));
                const float inv = 1.0f / LL;
                const float v0 = quad_sum(fmaf(w0, bf_lo(g0.x), w1_ * bf_lo(g1.x))) * inv, v1 = quad_sum(fmaf(w0, bf_hi(g0.x), w1_ * bf_hi(g1.x))) * inv;
                const float v2 = quad_sum(fmaf(w0, bf_lo(g0.z), w1_ * bf_lo(g1.z))) * inv, v3 = quad_sum(fmaf(w0, bf_hi(g0.z), w1_ * bf_hi(g1.z))) * inv;
                af = u32x2{pack_bf16(v0, v1), pack_bf16(v2, v3)};
            } else {
                const int offh = GH_OFF + (((4 * rg2 + (wave - 4)) * 4 + lb) * 8 + 2 * g) * 8;
                u32x4 gv;
                for (unsigned spins = 0;;) {
                    CBAR();
                    gv = __builtin_amdgcn_raw_buffer_load_b128(rs, offh, 0, SC1);
                    bool ok = (gv.y == tag) & (gv.w == tag);
                    if (__all(ok) || ++spins > SPIN_CAP) break;
                    __builtin_amdgcn_s_sleep(H2_SLEEP);
                }
                af = u32x2{gv.x, gv.z};
            }
            TS(7);
            f32x4 z = {0.f, 0.f, 0.f, 0.f};
            float o8[8];
#pragma unroll
            for (int e = 0; e < 8; ++e) o8[e] = mfma16(af, bfr[e], z)[0];
            float* dst = reinterpret_cast<float*>(smem + L_WP) + (wave * 4 + g) * 128 + c * 8;
            *reinterpret_cast<f32x4*>(dst) = f32x4{o8[0], o8[1], o8[2], o8[3]};
            *reinterpret_cast<f32x4*>(dst + 4) = f32x4{o8[4], o8[5], o8[6], o8[7]};
        }
        __syncthreads();
        if (has2) {
            const int t8 = tid & 255, r = t8 >> 2, b = t8 & 3, wb = wave & 4;
            float a0 = 0.f, a1 = 0.f;
#pragma unroll
            for (int w = 0; w < 4; ++w) {
                f32x2 t = *reinterpret_cast<const f32x2*>(smem + L_WP + (((wb + w) * 4 + b) * 128 + 2 * r) * 4);
                a0 += t.x; a1 += t.y;
            }
            const int cgx = (wave < 4) ? cgp : cg2, slot3 = (wave < 4) ? rg : 64 + rg2;
            u32x4 gv = {__float_as_uint(a0), tag, __float_as_uint(a1), tag};
            __builtin_amdgcn_raw_buffer_store_b128(gv, rs, G3_OFF + ((cgx * 96 + slot3) * 64 * 8 + t8 * 2) * 8, 0, SC1);
        }
        TS(8);
        TS_FLUSH();
    } else {
        u32x4 xv[4];
#pragma unroll
        for (int b = 0; b < 4; ++b) xv[b] = *reinterpret_cast<const u32x4*>(x + b * 512 + lane * 8);
        const u32x4 gwv = *reinterpret_cast<const u32x4*>(gw + lane * 8);
        const int cgf = bid >> 6, rr = bid & 63;
        const u32 b2v = *reinterpret_cast<const u32*>(b2 + 128 * cgf + 2 * rr);
        if (lane < 4) cnt[lane] = 0u;
        __builtin_amdgcn_sched_barrier(0);
        __syncthreads();
        __builtin_amdgcn_sched_barrier(0);
        const u32 tag = __builtin_amdgcn_readfirstlane(ev) + 1u;
        if (setA) {
            float mean[4], var[4];
#pragma unroll
            for (int b = 0; b < 4; ++b) {
                float s = ((bf_lo(xv[b][0]) + bf_hi(xv[b][0])) + (bf_lo(xv[b][1]) + bf_hi(xv[b][1]))) + ((bf_lo(xv[b][2]) + bf_hi(xv[b][2])) + (bf_lo(xv[b][3]) + bf_hi(xv[b][3])));
                mean[b] = wave_sum(s) * (1.0f / 512.0f);
            }
#pragma unroll
            for (int b = 0; b < 4; ++b) {
                float vs = 0.f;
#pragma unroll
                for (int e = 0; e < 4; ++e) { const float d0 = bf_lo(xv[b][e]) - mean[b], d1 = bf_hi(xv[b][e]) - mean[b]; vs = fmaf(d0, d0, vs); vs = fmaf(d1, d1, vs); }
                var[b] = wave_sum(vs) * (1.0f / 512.0f);
            }
            const float mn = (lane & 2) ? ((lane & 1) ? mean[3] : mean[2]) : ((lane & 1) ? mean[1] : mean[0]);
            const float vr = (lane & 2) ? ((lane & 1) ? var[3] : var[2]) : ((lane & 1) ? var[1] : var[0]);
            const float sd = rbf(sqrtf(rbf(rbf(vr) + 1e-5f)));
            if (lane < 4) *reinterpret_cast<f32x4*>(smem + L_YS + lane * 16) = f32x4{rbf(mn), sd, 1.0f / sd, 0.f};
            LDS_FENCE();
            if (lane == 0) __hip_atomic_store(cnt + 2, 1u, __ATOMIC_RELAXED, __HIP_MEMORY_SCOPE_WORKGROUP);
        }
        float gate[4];
#pragma unroll
        for (int b = 0; b < 4; ++b) {
            float s = 0.f;
#pragma unroll
            for (int e = 0; e < 4; ++e) { s += rbf(bf_lo(xv[b][e]) * bf_lo(gwv[e])); s += rbf(bf_hi(xv[b][e]) * bf_hi(gwv[e])); }
            s = rbf(wave_sum(s));
            gate[b] = rbf(1.0f / (1.0f + __expf(-s)));
        }
        TS(1);
        {
            const int off = GQ_OFF + (((j * 32 + (lane >> 1)) * 4 + ab) * 8 + (lane & 1) * 4) * 8;
            const u32 ldsdst = (u32)(uintptr_t)(__attribute__((address_space(3))) unsigned char*)(smem + L_QS) + lane * 16;
            u32 scnt;
#define PL(a, b) "buffer_load_dwordx4 v[" #a "], %[off], %[rs], 0 offen sc1\n\tbuffer_load_dwordx4 v[" #b "], %[off], %[rs], 0 offen offset:16 sc1\n\t"
#define PCHK(t0, t1, t2, t3, lab) "s_waitcnt vmcnt(6)\n\tv_xor_b32 v132, %[tag], v" #t0 "\n\tv_xor_b32 v133, %[tag], v" #t1 "\n\tv_xor_b32 v134, %[tag], v" #t2 "\n\tv_xor_b32 v135, %[tag], v" #t3 "\n\t" \
                   "v_or3_b32 v132, v132, v133, v134\n\tv_or_b32 v132, v132, v135\n\tv_cmp_ne_u32 vcc, 0, v132\n\ts_cbranch_vccz " lab "\n\t"
#define PDONE(lab, d0, d1, d2, d3) lab ":\n\tv_mov_b32 v132, v" #d0 "\n\tv_mov_b32 v133, v" #d1 "\n\tv_mov_b32 v134, v" #d2 "\n\tv_mov_b32 v135, v" #d3 "\n\ts_branch 20f\n\t"
            asm volatile(
                "s_mov_b32 %[cnt], 0\n\t"
                PL(136:139, 140:143) "s_sleep 5\n\t"
                PL(144:147, 148:151) "s_sleep 5\n\t"
                PL(152:155, 156:159) "s_sleep 5\n\t"
                PL(160:163, 164:167)
                "1:\n\t"
                PCHK(137, 139, 141, 143, "10f") PL(136:139, 140:143)
                PCHK(145, 147, 149, 151, "11f") PL(144:147, 148:151)
                PCHK(153, 155, 157, 159, "12f") PL(152:155, 156:159)
                PCHK(161, 163, 165, 167, "13f") PL(160:163, 164:167)
                "s_add_u32 %[cnt], %[cnt], 1\n\t"
                "s_cmp_lt_u32 %[cnt], %[cap]\n\t"
                "s_cbranch_scc1 1b\n\t"
                "s_waitcnt vmcnt(0)\n\t"
                PDONE("10", 136, 138, 140, 142)
                PDONE("11", 144, 146, 148, 150)
                PDONE("12", 152, 154, 156, 158)
                PDONE("13", 160, 162, 164, 166)
                "20:\n\t"
                "ds_write_b128 %[dst], v[132:135]\n\t"
                "s_waitcnt lgkmcnt(0)\n\t"
                : [cnt] "=&s"(scnt)
                : [off] "v"(off), [rs] "s"(rs), [tag] "s"(tag), [dst] "v"(ldsdst), [cap] "s"(SPIN_CAP)
                : "memory", "vcc", "scc", "v132", "v133", "v134", "v135",
                  "v136", "v137", "v138", "v139", "v140", "v141", "v142", "v143", "v144", "v145", "v146", "v147",
                  "v148", "v149", "v150", "v151", "v152", "v153", "v154", "v155", "v156", "v157", "v158", "v159",
                  "v160", "v161", "v162", "v163", "v164", "v165", "v166", "v167");
        }
        TS(3);
        __syncthreads();
        __syncthreads();
        __syncthreads();
        TS(4);
        {
            const int pp = lane >> 2, b = lane & 3;
            const int off = G3_OFF + (((cgf * 96 + pp) * 64 + rr) * 8 + 2 * b) * 8;
            u32x4 gv[6];
#pragma unroll
            for (int u = 0; u < 6; ++u) gv[u] = u32x4{0u, 0u, 0u, 0u};
            bool ok = false;
            for (unsigned spins = 0;;) {
                CBAR();
                if (!ok) {
#pragma unroll
                    for (int u = 0; u < 6; ++u) gv[u] = __builtin_amdgcn_raw_buffer_load_b128(rs, off + u * (16 * 4096), 0, SC1);
                    u32 bad = 0;
#pragma unroll
                    for (int u = 0; u < 5; ++u) bad |= (gv[u].y ^ tag) | (gv[u].w ^ tag);
                    if (pp < 12) bad |= (gv[5].y ^ tag) | (gv[5].w ^ tag);
                    ok = bad == 0u;
                }
                if (__all(ok) || ++spins > SPIN_CAP) break;
                __builtin_amdgcn_s_sleep(H3_SLEEP);
            }
            TS(5);
            float a0 = (__uint_as_float(gv[0].x) + __uint_as_float(gv[1].x)) + (__uint_as_float(gv[2].x) + __uint_as_float(gv[3].x));
            float a1 = (__uint_as_float(gv[0].z) + __uint_as_float(gv[1].z)) + (__uint_as_float(gv[2].z) + __uint_as_float(gv[3].z));
            float c0 = __uint_as_float(gv[4].x) + ((pp < 12) ? __uint_as_float(gv[5].x) : 0.f);
            float c1 = __uint_as_float(gv[4].z) + ((pp < 12) ? __uint_as_float(gv[5].z) : 0.f);
            a0 = xrow_sum(ror_sum(a0)); a1 = xrow_sum(ror_sum(a1)); c0 = xrow_sum(ror_sum(c0)); c1 = xrow_sum(ror_sum(c1));
            const float gt = (b & 2) ? ((b & 1) ? gate[3] : gate[2]) : ((b & 1) ? gate[1] : gate[0]);
            const float omg = rbf(1.0f - gt);
            const float mb0 = rbf(rbf(c0) + bf_lo(b2v)), mb1 = rbf(rbf(c1) + bf_hi(b2v));
            const float r0 = rbf(gt * rbf(a0)) + rbf(omg * mb0), r1 = rbf(gt * rbf(a1)) + rbf(omg * mb1);
            if (lane < 4) *reinterpret_cast<u32*>(out + b * 512 + 128 * cgf + 2 * rr) = pack_bf16(r0, r1);
            if (bid == 0 && lane == 0) __builtin_amdgcn_raw_buffer_store_b32(tag, rs, E_OFF, 0, SC1);
        }
        TS(6);
        TS_FLUSH();
    }
}

extern "C" void kernel_launch(void* const* d_in, const int* in_sizes, int n_in,
                              void* d_out, int out_size, void* d_ws, size_t ws_size,
                              hipStream_t stream) {
    fused_decode<<<256, 576, 0, stream>>>((const u16*)d_in[0], (const u16*)d_in[1], (const u16*)d_in[2],
                                          (const u16*)d_in[3], (const u16*)d_in[4], (const u16*)d_in[6],
                                          (const u16*)d_in[7], (const u16*)d_in[8], (const u16*)d_in[9],
                                          (const u16*)d_in[10], (u16*)d_out, (unsigned char*)d_ws);
}
```

```cpp
#include <hip/hip_runtime.h>
#include <hip/hip_bf16.h>
#include <stdint.h>

typedef unsigned short u16;
typedef unsigned int u32;
typedef u32 u32x2 __attribute__((ext_vector_type(2)));
typedef u32 u32x4 __attribute__((ext_vector_type(4)));
typedef float f32x2 __attribute__((ext_vector_type(2)));
typedef float f32x4 __attribute__((ext_vector_type(4)));
typedef short s16x4 __attribute__((ext_vector_type(4)));
typedef __bf16 bf16x8 __attribute__((ext_vector_type(8)));
typedef __bf16 bf16x2 __attribute__((ext_vector_type(2)));

#define NPOLL 1
#define WEARLY 4
#define QPRIO 0
#define VHOLD 1
#define QDELAY 260
#define VDELAY 0
#define QSLEEP 4
#define SCHED 1
#define H2_DELAY 24
#define H3_DELAY 20
#define H2_SLEEP 2
#define H3_SLEEP 1
#define H2_DIRECT 16
constexpr int NTHREADS = (8 + NPOLL) * 64;

constexpr int E_OFF = 0;
constexpr int GQ_OFF = 4096;
constexpr int GH_OFF = GQ_OFF + 262144;
constexpr int GO_OFF = GH_OFF + 131072;
constexpr int G3_OFF = GO_OFF + 1048576;
constexpr int WS_USED = G3_OFF + 4 * 96 * 64 * 64;
constexpr int OOB = 0x40000000;

constexpr int L_TP = 0;
constexpr int L_ST = L_TP + 8192;
constexpr int L_CNT = L_ST + 64;
constexpr int L_QS = L_CNT + 64;
constexpr int L_MX = L_QS + 1024;
constexpr int L_LS = L_MX + 128;
constexpr int L_OP = L_LS + 128;
constexpr int L_WP = L_OP + 16384;
constexpr int L_END = L_WP + 16384;
constexpr int LDS_BYTES = 84 * 1024;
static_assert(L_END <= LDS_BYTES, "lds");

constexpr unsigned SPIN_CAP = 1u << 18;
constexpr float LOG2E = 1.4426950408889634f;

__device__ __forceinline__ float bf_lo(u32 v) { return __uint_as_float(v << 16); }
__device__ __forceinline__ float bf_hi(u32 v) { return __uint_as_float(v & 0xffff0000u); }
__device__ __forceinline__ u32 pack_bf16(float lo, float hi) {
    bf16x2 v = {(__bf16)lo, (__bf16)hi};
    return __builtin_bit_cast(u32, v);
}
__device__ __forceinline__ float rbf(float f) { return bf_lo((u32)__builtin_bit_cast(u16, (__bf16)f)); }

template <int CTRL> __device__ __forceinline__ float dppf(float x) {
    return __builtin_bit_cast(float, __builtin_amdgcn_mov_dpp(__builtin_bit_cast(int, x), CTRL, 0xf, 0xf, true));
}
constexpr int XOR1 = 0xB1, XOR2 = 0x4E, ROR4 = 0x124, ROR8 = 0x128;
__device__ __forceinline__ float quad_sum(float x) { x += dppf<XOR1>(x); x += dppf<XOR2>(x); return x; }
__device__ __forceinline__ float quad_max(float x) { x = fmaxf(x, dppf<XOR1>(x)); x = fmaxf(x, dppf<XOR2>(x)); return x; }
__device__ __forceinline__ float ror_sum(float x) { x += dppf<ROR8>(x); x += dppf<ROR4>(x); return x; }
__device__ __forceinline__ float xrow_max(float x) {
    auto s = __builtin_amdgcn_permlane16_swap(__float_as_uint(x), __float_as_uint(x), false, false);
    x = fmaxf(__uint_as_float(s[0]), __uint_as_float(s[1]));
    auto t = __builtin_amdgcn_permlane32_swap(__float_as_uint(x), __float_as_uint(x), false, false);
    return fmaxf(__uint_as_float(t[0]), __uint_as_float(t[1]));
}
__device__ __forceinline__ float xrow_sum(float x) {
    auto s = __builtin_amdgcn_permlane16_swap(__float_as_uint(x), __float_as_uint(x), false, false);
    x = __uint_as_float(s[0]) + __uint_as_float(s[1]);
    auto t = __builtin_amdgcn_permlane32_swap(__float_as_uint(x), __float_as_uint(x), false, false);
    return __uint_as_float(t[0]) + __uint_as_float(t[1]);
}
__device__ __forceinline__ float wave_sum(float x) { return xrow_sum(ror_sum(quad_sum(x))); }

__device__ __forceinline__ u32 plo(u32 a, u32 b) { return __builtin_amdgcn_perm(b, a, 0x05040100u); }
__device__ __forceinline__ u32 phi(u32 a, u32 b) { return __builtin_amdgcn_perm(b, a, 0x07060302u); }

__device__ __forceinline__ f32x4 mfma16(u32x2 a, u32x2 b, f32x4 c) {
    return __builtin_amdgcn_mfma_f32_16x16x16bf16_1k(__builtin_bit_cast(s16x4, a), __builtin_bit_cast(s16x4, b), c, 0, 0, 0);
}
__device__ __forceinline__ f32x4 mfma32(u32x4 a, u32x4 b, f32x4 c) {
    return __builtin_amdgcn_mfma_f32_16x16x32_bf16(__builtin_bit_cast(bf16x8, a), __builtin_bit_cast(bf16x8, b), c, 0, 0, 0);
}
__device__ __forceinline__ float sel4(f32x4 d, int s) {
    float lo = (s & 1) ? d[1] : d[0], hi = (s & 1) ? d[3] : d[2];
    return (s & 2) ? hi : lo;
}
__device__ __forceinline__ void transpose4x8(const u32x4 (&w)[4], u32x2 (&f)[8]) {
#pragma unroll
    for (int r = 0; r < 4; ++r) {
        f[2 * r] = u32x2{plo(w[0][r], w[1][r]), plo(w[2][r], w[3][r])};
        f[2 * r + 1] = u32x2{phi(w[0][r], w[1][r]), phi(w[2][r], w[3][r])};
    }
}
#define SC1 16
#define NT 2
#define CBAR() asm volatile("" ::: "memory")
#define KEEP4(v) asm volatile("" : "+v"((v).x), "+v"((v).y), "+v"((v).z), "+v"((v).w))
#define LDS_FENCE() asm volatile("s_waitcnt lgkmcnt(0)" ::: "memory")
#define LDS_LD(p) __hip_atomic_load((p), __ATOMIC_RELAXED, __HIP_MEMORY_SCOPE_WORKGROUP)
#define SB() __builtin_amdgcn_sched_barrier(0)

#define TS(k)
#define TS_DECL
#define TS_FLUSH()

__global__ void __launch_bounds__(NTHREADS, 3)
fused_decode(const u16* __restrict__ x, const u16* __restrict__ kc, const u16* __restrict__ vc,
             const u16* __restrict__ wq, const u16* __restrict__ wo, const u16* __restrict__ w1,
             const u16* __restrict__ b1, const u16* __restrict__ w2, const u16* __restrict__ b2,
             const u16* __restrict__ gw, u16* __restrict__ out, unsigned char* ws) {
    __shared__ __attribute__((aligned(16))) unsigned char smem[LDS_BYTES];
    const int tid = threadIdx.x, lane = tid & 63;
    const int wave = __builtin_amdgcn_readfirstlane(tid >> 6);
    const int bid = blockIdx.x, j = bid & 7, i = bid >> 3;
    const bool setA = i < 14, setB = (i >= 14) && (i < 28);
    const int g = lane >> 4, c = lane & 15;
    const __amdgpu_buffer_rsrc_t rs = __builtin_amdgcn_make_buffer_rsrc(ws, 0, WS_USED, 0x00020000);
    const int ab = i >> 3, as = i & 7;
    const int rg = 8 * j + (i >> 2), cgp = i & 3;
    const int tb = j * 14 + (i - 14), rg2 = tb >> 2, cg2 = tb & 3;
    u32* const cnt = reinterpret_cast<u32*>(smem + L_CNT);
    TS_DECL
    TS(0);
    const unsigned long long tstart = __builtin_amdgcn_s_memrealtime();

    if (wave < 8) {
        const bool lowh = wave < 4;
        const int w4 = wave & 3;
        const int ta = j * 14 + i;
        const int kq = lowh ? (i >> 3) : (ta & 3), cb = lowh ? (i & 7) : (ta >> 2);
        const int r0 = 128 * kq + 32 * w4;
        const __amdgpu_buffer_rsrc_t r1 = __builtin_amdgcn_make_buffer_rsrc((void*)(lowh ? wq : w1), 0, lowh ? 512 * 4096 * 2 : 512 * 1792 * 2, 0x00020000);
        const int ldw = lowh ? 8192 : 3584;
        const int off1 = (lowh || setA) ? (r0 + (g * 2 + (c >> 3)) * 4) * ldw + ((lowh ? 512 * j : 0) + 64 * cb + (c & 7) * 8) * 2 : OOB;
        u32x4 t1[4], t2[4];
#pragma unroll
        for (int t = 0; t < 4; ++t) t1[t] = __builtin_amdgcn_raw_buffer_load_b128(r1, (lowh ? off1 : OOB) + t * 8192, 0, NT);
        const u32x2 xa = *reinterpret_cast<const u32x2*>(x + (c >> 2) * 512 + r0 + (g * 2 + ((c >> 1) & 1)) * 4);
        const bool has2 = lowh || setB;
        const __amdgpu_buffer_rsrc_t r2 = __builtin_amdgcn_make_buffer_rsrc((void*)(lowh ? wo : w2), 0, lowh ? 4096 * 512 * 2 : 1792 * 512 * 2, 0x00020000);
        const int off2 = has2 ? ((64 * (lowh ? rg : rg2) + 16 * w4 + 4 * g) * 512 + 128 * (lowh ? cgp : cg2) + 8 * c) * 2 : OOB;
        u32x4 kf[4], vf[4], wf[4];
        const u32 ev = __builtin_amdgcn_raw_buffer_load_b32(rs, E_OFF, 0, SC1);
        const __amdgpu_buffer_rsrc_t rb1 = __builtin_amdgcn_make_buffer_rsrc((void*)b1, 0, 1792 * 2, 0x00020000);
        const u32x2 b1v = __builtin_amdgcn_raw_buffer_load_b64(rb1, (!lowh && setB) ? (64 * rg2 + 16 * w4 + 4 * g) * 2 : OOB, 0, 0);
        const u32 b2v = *reinterpret_cast<const u32*>(b2 + 64 * j + 2 * i);
        const __amdgpu_buffer_rsrc_t rk = __builtin_amdgcn_make_buffer_rsrc((void*)kc, 0, 32 << 20, 0x00020000);
        const __amdgpu_buffer_rsrc_t rv = __builtin_amdgcn_make_buffer_rsrc((void*)vc, 0, 32 << 20, 0x00020000);
        const int kvb = ((ab * 8 + j) * 4096 + 3072 + 128 * as + 16 * wave) * 256;
        const int offk = kvb + (4 * ((lane >> 2) & 3)) * 256 + (g * 4 + (lane & 3)) * 16;
        const int offv = kvb + (4 * g) * 256 + c * 16;
        SB();
#pragma unroll
        for (int t = 0; t < 4; ++t) t2[t] = __builtin_amdgcn_raw_buffer_load_b128(r1, (lowh ? OOB : off1) + t * 3584, 0, NT);
#pragma unroll
        for (int t = 0; t < WEARLY; ++t) wf[t] = __builtin_amdgcn_raw_buffer_load_b128(r2, off2 + t * 1024, 0, NT);
        SB();
        __syncthreads();
        SB();
        const u32 tag = __builtin_amdgcn_readfirstlane(ev) + 1u;

        auto tile_phase = [&](const u32x4 (&tw)[4], u32x2 af, int cidx, int goff) {
            u32x2 f[8];
            transpose4x8(tw, f);
            const f32x4 z = {0.f, 0.f, 0.f, 0.f};
            float v[8];
#pragma unroll
            for (int e = 0; e < 8; ++e) { const f32x4 d = mfma16(af, f[e], z); const float s = (c & 8) ? d[2] : d[0]; v[e] = s + dppf<ROR8>(s); }
            if (c < 8) {
                float* dst = reinterpret_cast<float*>(smem + L_TP) + (wave * 4 + g) * 64 + c * 8;
                *reinterpret_cast<f32x4*>(dst) = f32x4{v[0], v[1], v[2], v[3]};
                *reinterpret_cast<f32x4*>(dst + 4) = f32x4{v[4], v[5], v[6], v[7]};
            }
            LDS_FENCE();
            u32 old = 0;
            if (lane == 0) old = __hip_atomic_fetch_add(cnt + cidx, 1u, __ATOMIC_RELAXED, __HIP_MEMORY_SCOPE_WORKGROUP);
            old = __builtin_amdgcn_readfirstlane(old);
            if (old == 3u) {
                CBAR();
                const float* src = reinterpret_cast<const float*>(smem + L_TP) + (wave & 4) * 256 + lane * 4;
                f32x4 a = *reinterpret_cast<const f32x4*>(src);
#pragma unroll
                for (int w = 1; w < 4; ++w) a += *reinterpret_cast<const f32x4*>(src + w * 256);
                const u32x4 gv = {pack_bf16(a[0], a[1]), tag, pack_bf16(a[2], a[3]), tag};
                __builtin_amdgcn_raw_buffer_store_b128(gv, rs, goff + lane * 16, 0, SC1);
                if (cidx == 0) TS(10);
            }
        };
        if (lowh) tile_phase(t1, xa, 0, GQ_OFF + ((j * 8 + cb) * 4 + kq) * 1024);
        TS(1);
        while (LDS_LD(cnt + 0) < 4u) __builtin_amdgcn_s_sleep(1);
        CBAR();
#pragma unroll
        for (int t = 0; t < 4; ++t) kf[t] = __builtin_amdgcn_raw_buffer_load_b128(rk, offk + t * 256, 0, NT);
        SB();
        KEEP4(kf[0]); KEEP4(kf[1]);
        while (LDS_LD(cnt + 6) == 0u) __builtin_amdgcn_s_sleep(1);
        CBAR();
        vf[0] = __builtin_amdgcn_raw_buffer_load_b128(rv, offv, 0, NT); vf[1] = __builtin_amdgcn_raw_buffer_load_b128(rv, offv + 256, 0, NT);
        SB();
        KEEP4(kf[2]); KEEP4(kf[3]);
        vf[2] = __builtin_amdgcn_raw_buffer_load_b128(rv, offv + 512, 0, NT); vf[3] = __builtin_amdgcn_raw_buffer_load_b128(rv, offv + 768, 0, NT);
        SB();
        TS(2);
        if (!lowh && setA) {
            while (LDS_LD(cnt + 2) == 0u) __builtin_amdgcn_s_sleep(1);
            CBAR();
            const f32x4 stt = *reinterpret_cast<const f32x4*>(smem + L_ST + (c >> 2) * 16);
            float d[4] = {rbf(bf_lo(xa.x) - stt[0]), rbf(bf_hi(xa.x) - stt[0]), rbf(bf_lo(xa.y) - stt[0]), rbf(bf_hi(xa.y) - stt[0])};
#pragma unroll
            for (int e = 0; e < 4; ++e) { const float q0 = d[e] * stt[2]; d[e] = fmaf(fmaf(-q0, stt[1], d[e]), stt[2], q0); }
            tile_phase(t2, u32x2{pack_bf16(d[0], d[1]), pack_bf16(d[2], d[3])}, 1, GH_OFF + (cb * 4 + kq) * 1024);
        }
        TS(3);
        while (LDS_LD(cnt + 3) == 0u) __builtin_amdgcn_s_sleep(1);
        CBAR();
        TS(4);
#pragma unroll
        for (int t = WEARLY; t < 4; ++t) wf[t] = __builtin_amdgcn_raw_buffer_load_b128(r2, off2 + t * 1024, 0, NT);
        SB();
        float sc[4];
        {
            const u32x4 qf = *reinterpret_cast<const u32x4*>(smem + L_QS + (((lane >> 2) & 3) * 128 + (g * 4 + (lane & 3)) * 8) * 2);
            const f32x4 z = {0.f, 0.f, 0.f, 0.f};
#pragma unroll
            for (int t = 0; t < 4; ++t) {
                const f32x4 d = mfma32(kf[t], qf, z);
                sc[t] = rbf(rbf(quad_sum(sel4(d, lane & 3))) * 0.08838834764831845f);
            }
            const float m = xrow_max(fmaxf(fmaxf(sc[0], sc[1]), fmaxf(sc[2], sc[3])));
            if (g == 0 && (c & 3) == 0) reinterpret_cast<float*>(smem + L_MX)[(c >> 2) * 8 + wave] = m;
        }
        LDS_FENCE();
        if (lane == 0) __hip_atomic_fetch_add(cnt + 4, 1u, __ATOMIC_RELAXED, __HIP_MEMORY_SCOPE_WORKGROUP);
        while (LDS_LD(cnt + 4) < 8u) {}
        CBAR();
        float M;
        u32x2 pa;
        {
            const f32x4 ma = *reinterpret_cast<const f32x4*>(smem + L_MX + (c >> 2) * 32), mb = *reinterpret_cast<const f32x4*>(smem + L_MX + (c >> 2) * 32 + 16);
            M = fmaxf(fmaxf(fmaxf(ma[0], ma[1]), fmaxf(ma[2], ma[3])), fmaxf(fmaxf(mb[0], mb[1]), fmaxf(mb[2], mb[3])));
            const float p0 = __builtin_amdgcn_exp2f((sc[0] - M) * LOG2E), p1 = __builtin_amdgcn_exp2f((sc[1] - M) * LOG2E);
            const float p2 = __builtin_amdgcn_exp2f((sc[2] - M) * LOG2E), p3 = __builtin_amdgcn_exp2f((sc[3] - M) * LOG2E);
            pa = u32x2{pack_bf16(p0, p1), pack_bf16(p2, p3)};
            const float l = xrow_sum((p0 + p1) + (p2 + p3));
            if (g == 0 && (c & 3) == 0) reinterpret_cast<float*>(smem + L_LS)[(c >> 2) * 8 + wave] = l;
        }
        TS(5);
        {
            u32x2 f[8];
            transpose4x8(vf, f);
            const f32x4 z = {0.f, 0.f, 0.f, 0.f};
            float o8[8];
#pragma unroll
            for (int e = 0; e < 8; ++e) o8[e] = mfma16(pa, f[e], z)[0];
            float* dst = reinterpret_cast<float*>(smem + L_OP) + (wave * 4 + g) * 128 + c * 8;
            *reinterpret_cast<f32x4*>(dst) = f32x4{o8[0], o8[1], o8[2], o8[3]};
            *reinterpret_cast<f32x4*>(dst + 4) = f32x4{o8[4], o8[5], o8[6], o8[7]};
        }
        __syncthreads();
        if (wave >= 4) {
            const int hl = wave - 4, pc = lane & 7;
            const float* src = reinterpret_cast<const float*>(smem + L_OP) + hl * 128 + (lane >> 3) * 16 + (pc & 3) * 4;
            f32x4 a = *reinterpret_cast<const f32x4*>(src);
#pragma unroll
            for (int w = 1; w < 8; ++w) a += *reinterpret_cast<const f32x4*>(src + w * 512);
            const f32x4 la = *reinterpret_cast<const f32x4*>(smem + L_LS + hl * 32), lb_ = *reinterpret_cast<const f32x4*>(smem + L_LS + hl * 32 + 16);
            const f32x4 ma = *reinterpret_cast<const f32x4*>(smem + L_MX + hl * 32), mb = *reinterpret_cast<const f32x4*>(smem + L_MX + hl * 32 + 16);
            const float L = ((la[0] + la[1]) + (la[2] + la[3])) + ((lb_[0] + lb_[1]) + (lb_[2] + lb_[3]));
            const float Mh = fmaxf(fmaxf(fmaxf(ma[0], ma[1]), fmaxf(ma[2], ma[3])), fmaxf(fmaxf(mb[0], mb[1]), fmaxf(mb[2], mb[3])));
            const u32x4 gv = {pc < 4 ? pack_bf16(a[0], a[1]) : __float_as_uint(Mh), tag, pc < 4 ? pack_bf16(a[2], a[3]) : __float_as_uint(L), tag};
            __builtin_amdgcn_raw_buffer_store_b128(gv, rs, pc <= 4 ? GO_OFF + ((((ab * 8 + j) * 8 + as) * 4 + hl) * 64 + lane) * 16 : OOB, 0, SC1);
            if (wave == 7) TS(11);
        }
        TS(6);
        if (has2) {
            u32x2 bfr[8];
            transpose4x8(wf, bfr);
#pragma unroll
            for (int e = 0; e < 8; ++e) asm volatile("" : "+v"(bfr[e].x), "+v"(bfr[e].y));
            u32x2 af;
            const int lb = c >> 2, sq = c & 3;
            if (lowh) {
                const int head = rg >> 1, hl = head & 3;
                const int offl = GO_OFF + ((((lb * 8 + j) * 8 + sq) * 4 + hl) * 8 + 4 * (rg & 1) + w4) * 128;
                u32x4 g0 = {0u, 0u, 0u, 0u}, g1 = g0, m0 = g0, m1 = g0;
                bool ok = false;
                __builtin_amdgcn_s_sleep(H2_DELAY);
                for (unsigned spins = 0;;) {
                    CBAR();
                    if (!ok) {
                        g0 = __builtin_amdgcn_raw_buffer_load_b128(rs, offl + g * 16, 0, SC1);
                        g1 = __builtin_amdgcn_raw_buffer_load_b128(rs, offl + g * 16 + 16384, 0, SC1);
                        m0 = __builtin_amdgcn_raw_buffer_load_b128(rs, offl + 64, 0, SC1);
                        m1 = __builtin_amdgcn_raw_buffer_load_b128(rs, offl + 64 + 16384, 0, SC1);
                        ok = ((((g0.y ^ tag) | (g0.w ^ tag)) | ((g1.y ^ tag) | (g1.w ^ tag))) | (((m0.y ^ tag) | (m0.w ^ tag)) | ((m1.y ^ tag) | (m1.w ^ tag)))) == 0u;
                    }
                    if (__all(ok) || ++spins > SPIN_CAP) break;
                    __builtin_amdgcn_s_sleep(H2_SLEEP);
                }
                const float ma = __uint_as_float(m0.x), mb = __uint_as_float(m1.x);
                const float MM = quad_max(fmaxf(ma, mb));
                const float w0 = __builtin_amdgcn_exp2f((ma - MM) * LOG2E), w1_ = __builtin_amdgcn_exp2f((mb - MM) * LOG2E);
                const float LL = quad_sum(fmaf(w0, __uint_as_float(m0.z), w1_ * __uint_as_float(m1.z)));
                const float inv = 1.0f / LL;
                const float v0 = quad_sum(fmaf(w0, bf_lo(g0.x), w1_ * bf_lo(g1.x))) * inv, v1 = quad_sum(fmaf(w0, bf_hi(g0.x), w1_ * bf_hi(g1.x))) * inv;
                const float v2 = quad_sum(fmaf(w0, bf_lo(g0.z), w1_ * bf_lo(g1.z))) * inv, v3 = quad_sum(fmaf(w0, bf_hi(g0.z), w1_ * bf_hi(g1.z))) * inv;
                af = u32x2{pack_bf16(v0, v1), pack_bf16(v2, v3)};
            } else {
                const int offh = GH_OFF + ((rg2 * 4 + sq) * 4 + lb) * 256 + (8 * w4 + 2 * g) * 8;
                u32x4 gv;
                for (unsigned spins = 0;;) {
                    CBAR();
                    gv = __builtin_amdgcn_raw_buffer_load_b128(rs, offh, 0, SC1);
                    if (__all(((gv.y ^ tag) | (gv.w ^ tag)) == 0u) || ++spins > SPIN_CAP) break;
                    __builtin_amdgcn_s_sleep(H2_SLEEP);
                }
                float zz[4] = {quad_sum(bf_lo(gv.x)), quad_sum(bf_hi(gv.x)), quad_sum(bf_lo(gv.z)), quad_sum(bf_hi(gv.z))};
                const float bb[4] = {bf_lo(b1v.x), bf_hi(b1v.x), bf_lo(b1v.y), bf_hi(b1v.y)};
#pragma unroll
                for (int e = 0; e < 4; ++e) {
                    const float z = rbf(rbf(zz[e]) + bb[e]);
                    const float u = 0.7978845608028654f * (z + 0.044715f * z * z * z);
                    const float th = 1.0f - 2.0f / (__expf(2.0f * u) + 1.0f);
                    zz[e] = 0.5f * z * (1.0f + th);
                }
                af = u32x2{pack_bf16(zz[0], zz[1]), pack_bf16(zz[2], zz[3])};
            }
            TS(7);
            const f32x4 z = {0.f, 0.f, 0.f, 0.f};
            float o8[8];
#pragma unroll
            for (int e = 0; e < 8; ++e) o8[e] = mfma16(af, bfr[e], z)[0];
            float* dst = reinterpret_cast<float*>(smem + L_WP) + (wave * 4 + g) * 128 + c * 8;
            *reinterpret_cast<f32x4*>(dst) = f32x4{o8[0], o8[1], o8[2], o8[3]};
            *reinterpret_cast<f32x4*>(dst + 4) = f32x4{o8[4], o8[5], o8[6], o8[7]};
        }
        __syncthreads();
        if (has2) {
            const int t8 = tid & 255, r = t8 >> 2, b = t8 & 3, wb = wave & 4;
            float a0 = 0.f, a1 = 0.f;
#pragma unroll
            for (int w = 0; w < 4; ++w) {
                const f32x2 t = *reinterpret_cast<const f32x2*>(smem + L_WP + (((wb + w) * 4 + b) * 128 + 2 * r) * 4);
                a0 += t.x; a1 += t.y;
            }
            const int cgx = lowh ? cgp : cg2, slot3 = lowh ? rg : 64 + rg2;
            const u32x4 gv = {__float_as_uint(a0), tag, __float_as_uint(a1), tag};
            __builtin_amdgcn_raw_buffer_store_b128(gv, rs, G3_OFF + ((cgx * 96 + slot3) * 64 * 8 + t8 * 2) * 8, 0, SC1);
        }
        TS(8);
        if (wave == 0) {
            const int cgf = j >> 1, rr = (j & 1) * 32 + i;
            const int pp = lane >> 2, b = lane & 3;
            const float gt = reinterpret_cast<const float*>(smem + L_ST)[b * 4 + 3];
            const int off = G3_OFF + (((cgf * 96 + pp) * 64 + rr) * 8 + 2 * b) * 8;
            u32x4 gv[6];
#pragma unroll
            for (int u = 0; u < 6; ++u) gv[u] = u32x4{0u, 0u, 0u, 0u};
            bool ok = false;
            __builtin_amdgcn_s_sleep(H3_DELAY);
            for (unsigned spins = 0;;) {
                CBAR();
                if (!ok) {
#pragma unroll
                    for (int u = 0; u < 6; ++u) gv[u] = __builtin_amdgcn_raw_buffer_load_b128(rs, off + u * (16 * 4096), 0, SC1);
                    u32 bad = 0;
#pragma unroll
                    for (int u = 0; u < 5; ++u) bad |= (gv[u].y ^ tag) | (gv[u].w ^ tag);
                    if (pp < 12) bad |= (gv[5].y ^ tag) | (gv[5].w ^ tag);
                    ok = bad == 0u;
                }
                if (__all(ok) || ++spins > SPIN_CAP) break;
                __builtin_amdgcn_s_sleep(H3_SLEEP);
            }
            float a0 = (__uint_as_float(gv[0].x) + __uint_as_float(gv[1].x)) + (__uint_as_float(gv[2].x) + __uint_as_float(gv[3].x));
            float a1 = (__uint_as_float(gv[0].z) + __uint_as_float(gv[1].z)) + (__uint_as_float(gv[2].z) + __uint_as_float(gv[3].z));
            float c0 = __uint_as_float(gv[4].x) + ((pp < 12) ? __uint_as_float(gv[5].x) : 0.f);
            float c1 = __uint_as_float(gv[4].z) + ((pp < 12) ? __uint_as_float(gv[5].z) : 0.f);
            a0 = xrow_sum(ror_sum(a0)); a1 = xrow_sum(ror_sum(a1)); c0 = xrow_sum(ror_sum(c0)); c1 = xrow_sum(ror_sum(c1));
            const float omg = rbf(1.0f - gt);
            const float mb0 = rbf(rbf(c0) + bf_lo(b2v)), mb1 = rbf(rbf(c1) + bf_hi(b2v));
            const float r0_ = rbf(gt * rbf(a0)) + rbf(omg * mb0), r1_ = rbf(gt * rbf(a1)) + rbf(omg * mb1);
            if (lane < 4) *reinterpret_cast<u32*>(out + b * 512 + 64 * j + 2 * i) = pack_bf16(r0_, r1_);
            if (bid == 0 && lane == 0) __builtin_amdgcn_raw_buffer_store_b32(tag, rs, E_OFF, 0, SC1);
            TS(9);
        }
        TS_FLUSH();
    } else {
        const u32 ev = __builtin_amdgcn_raw_buffer_load_b32(rs, E_OFF, 0, SC1);
        if (wave == 8) {
            u32x4 xv[4];
#pragma unroll
            for (int b = 0; b < 4; ++b) xv[b] = *reinterpret_cast<const u32x4*>(x + b * 512 + lane * 8);
            const u32x4 gwv = *reinterpret_cast<const u32x4*>(gw + lane * 8);
            if (lane < 8) cnt[lane] = 0u;
            SB();
            __syncthreads();
            SB();
            float mean[4], var[4], gs[4];
#pragma unroll
            for (int b = 0; b < 4; ++b) {
                const float s = ((bf_lo(xv[b][0]) + bf_hi(xv[b][0])) + (bf_lo(xv[b][1]) + bf_hi(xv[b][1]))) + ((bf_lo(xv[b][2]) + bf_hi(xv[b][2])) + (bf_lo(xv[b][3]) + bf_hi(xv[b][3])));
                mean[b] = wave_sum(s) * (1.0f / 512.0f);
            }
#pragma unroll
            for (int b = 0; b < 4; ++b) {
                float vs = 0.f, s = 0.f;
#pragma unroll
                for (int e = 0; e < 4; ++e) {
                    const float d0 = bf_lo(xv[b][e]) - mean[b], d1 = bf_hi(xv[b][e]) - mean[b]; vs = fmaf(d0, d0, vs); vs = fmaf(d1, d1, vs);
                    s += rbf(bf_lo(xv[b][e]) * bf_lo(gwv[e])); s += rbf(bf_hi(xv[b][e]) * bf_hi(gwv[e]));
                }
                var[b] = wave_sum(vs) * (1.0f / 512.0f);
                gs[b] = wave_sum(s);
            }
            const float mn = (lane & 2) ? ((lane & 1) ? mean[3] : mean[2]) : ((lane & 1) ? mean[1] : mean[0]);
            const float vr = (lane & 2) ? ((lane & 1) ? var[3] : var[2]) : ((lane & 1) ? var[1] : var[0]);
            const float gg = rbf((lane & 2) ? ((lane & 1) ? gs[3] : gs[2]) : ((lane & 1) ? gs[1] : gs[0]));
            const float sd = rbf(sqrtf(rbf(rbf(vr) + 1e-5f)));
            if (lane < 4) *reinterpret_cast<f32x4*>(smem + L_ST + lane * 16) = f32x4{rbf(mn), sd, 1.0f / sd, rbf(1.0f / (1.0f + __expf(-gg)))};
            LDS_FENCE();
            if (lane == 0) __hip_atomic_store(cnt + 2, 1u, __ATOMIC_RELAXED, __HIP_MEMORY_SCOPE_WORKGROUP);
        } else {
            SB();
            __syncthreads();
            SB();
        }
        TS(1);
        const u32 tag = __builtin_amdgcn_readfirstlane(ev) + 1u;
        while (LDS_LD(cnt + 0) < 4u) __builtin_amdgcn_s_sleep(2);
        while (__builtin_amdgcn_s_memrealtime() - tstart < QDELAY) __builtin_amdgcn_s_sleep(1);
        if (wave > 8) __builtin_amdgcn_s_sleep(QSLEEP * 2);
        if (wave > 9) __builtin_amdgcn_s_sleep(QSLEEP * 2);
        const int off = GQ_OFF + (((j * 8 + (lane >> 3)) * 4) * 4 + ab) * 256 + (lane & 7) * 32;
        for (unsigned spins = 0;; ++spins) {
            CBAR();
            u32x4 pa[4], pb[4];
#pragma unroll
            for (int k = 0; k < 4; ++k) { pa[k] = __builtin_amdgcn_raw_buffer_load_b128(rs, off + k * 1024, 0, SC1); pb[k] = __builtin_amdgcn_raw_buffer_load_b128(rs, off + k * 1024 + 16, 0, SC1); }
            SB();
            if (spins == 0 && lane == 0) __hip_atomic_store(cnt + 6, 1u, __ATOMIC_RELAXED, __HIP_MEMORY_SCOPE_WORKGROUP);
            SB();
            u32 bad = 0;
#pragma unroll
            for (int k = 0; k < 4; ++k) bad |= ((pa[k].y ^ tag) | (pa[k].w ^ tag)) | ((pb[k].y ^ tag) | (pb[k].w ^ tag));
            if (__all(bad == 0u) || spins > SPIN_CAP) {
                u32x4 qv;
                qv[0] = pack_bf16((bf_lo(pa[0].x) + bf_lo(pa[1].x)) + (bf_lo(pa[2].x) + bf_lo(pa[3].x)), (bf_hi(pa[0].x) + bf_hi(pa[1].x)) + (bf_hi(pa[2].x) + bf_hi(pa[3].x)));
                qv[1] = pack_bf16((bf_lo(pa[0].z) + bf_lo(pa[1].z)) + (bf_lo(pa[2].z) + bf_lo(pa[3].z)), (bf_hi(pa[0].z) + bf_hi(pa[1].z)) + (bf_hi(pa[2].z) + bf_hi(pa[3].z)));
                qv[2] = pack_bf16((bf_lo(pb[0].x) + bf_lo(pb[1].x)) + (bf_lo(pb[2].x) + bf_lo(pb[3].x)), (bf_hi(pb[0].x) + bf_hi(pb[1].x)) + (bf_hi(pb[2].x) + bf_hi(pb[3].x)));
                qv[3] = pack_bf16((bf_lo(pb[0].z) + bf_lo(pb[1].z)) + (bf_lo(pb[2].z) + bf_lo(pb[3].z)), (bf_hi(pb[0].z) + bf_hi(pb[1].z)) + (bf_hi(pb[2].z) + bf_hi(pb[3].z)));
                *reinterpret_cast<u32x4*>(smem + L_QS + lane * 16) = qv;
                LDS_FENCE();
                if (lane == 0) __hip_atomic_store(cnt + 3, 1u, __ATOMIC_RELAXED, __HIP_MEMORY_SCOPE_WORKGROUP);
                break;
            }
        }
        TS(3);
        TS_FLUSH();
    }
}

extern "C" void kernel_launch(void* const* d_in, const int* in_sizes, int n_in,
                              void* d_out, int out_size, void* d_ws, size_t ws_size,
                              hipStream_t stream) {
    fused_decode<<<256, NTHREADS, 0, stream>>>((const u16*)d_in[0], (const u16*)d_in[1], (const u16*)d_in[2],
                                               (const u16*)d_in[3], (const u16*)d_in[4], (const u16*)d_in[6],
                                               (const u16*)d_in[7], (const u16*)d_in[8], (const u16*)d_in[9],
                                               (const u16*)d_in[10], (u16*)d_out, (unsigned char*)d_ws);
}
```

```cpp
#include <hip/hip_runtime.h>
#include <hip/hip_bf16.h>
#include <stdint.h>

typedef unsigned short u16;
typedef unsigned int u32;
typedef u32 u32x2 __attribute__((ext_vector_type(2)));
typedef u32 u32x4 __attribute__((ext_vector_type(4)));
typedef float f32x2 __attribute__((ext_vector_type(2)));
typedef float f32x4 __attribute__((ext_vector_type(4)));
typedef short s16x4 __attribute__((ext_vector_type(4)));
typedef __bf16 bf16x8 __attribute__((ext_vector_type(8)));
typedef __bf16 bf16x2 __attribute__((ext_vector_type(2)));

#define NPOLL 1
#define WEARLY 4
#define QPRIO 0
#define VHOLD 1
#define FINW 2
#define QDELAY 340
#define VDELAY 0
#define QSLEEP 4
#define SCHED 1
#define H2_DELAY 24
#define H3_DELAY 20
#define H2_SLEEP 2
#define H3_SLEEP 1
#define H2_DIRECT 16
constexpr int NTHREADS = 13 * 64;

constexpr int E_OFF = 0;
constexpr int GQ_OFF = 4096;
constexpr int GH_OFF = GQ_OFF + 262144;
constexpr int GO_OFF = GH_OFF + 131072;
constexpr int G3_OFF = GO_OFF + 1048576;
constexpr int WS_USED = G3_OFF + 4 * 96 * 64 * 32;
constexpr int OOB = 0x40000000;

constexpr int L_TP = 0;
constexpr int L_ST = L_TP + 8192;
constexpr int L_CNT = L_ST + 64;
constexpr int L_QS = L_CNT + 64;
constexpr int L_MX = L_QS + 1024;
constexpr int L_LS = L_MX + 128;
constexpr int L_OP = L_LS + 128;
constexpr int L_WP = L_OP + 16384;
constexpr int L_END = L_WP + 16384;
constexpr int LDS_BYTES = 84 * 1024;
static_assert(L_END <= LDS_BYTES, "lds");

constexpr unsigned SPIN_CAP = 1u << 18;
constexpr float LOG2E = 1.4426950408889634f;

__device__ __forceinline__ float bf_lo(u32 v) { return __uint_as_float(v << 16); }
__device__ __forceinline__ float bf_hi(u32 v) { return __uint_as_float(v & 0xffff0000u); }
__device__ __forceinline__ u32 pack_bf16(float lo, float hi) {
    bf16x2 v = {(__bf16)lo, (__bf16)hi};
    return __builtin_bit_cast(u32, v);
}
__device__ __forceinline__ float rbf(float f) { return bf_lo((u32)__builtin_bit_cast(u16, (__bf16)f)); }

template <int CTRL> __device__ __forceinline__ float dppf(float x) {
    return __builtin_bit_cast(float, __builtin_amdgcn_mov_dpp(__builtin_bit_cast(int, x), CTRL, 0xf, 0xf, true));
}
constexpr int XOR1 = 0xB1, XOR2 = 0x4E, ROR4 = 0x124, ROR8 = 0x128;
__device__ __forceinline__ float quad_sum(float x) { x += dppf<XOR1>(x); x += dppf<XOR2>(x); return x; }
__device__ __forceinline__ float quad_max(float x) { x = fmaxf(x, dppf<XOR1>(x)); x = fmaxf(x, dppf<XOR2>(x)); return x; }
__device__ __forceinline__ float ror_sum(float x) { x += dppf<ROR8>(x); x += dppf<ROR4>(x); return x; }
__device__ __forceinline__ float xrow_max(float x) {
    auto s = __builtin_amdgcn_permlane16_swap(__float_as_uint(x), __float_as_uint(x), false, false);
    x = fmaxf(__uint_as_float(s[0]), __uint_as_float(s[1]));
    auto t = __builtin_amdgcn_permlane32_swap(__float_as_uint(x), __float_as_uint(x), false, false);
    return fmaxf(__uint_as_float(t[0]), __uint_as_float(t[1]));
}
__device__ __forceinline__ float xrow_sum(float x) {
    auto s = __builtin_amdgcn_permlane16_swap(__float_as_uint(x), __float_as_uint(x), false, false);
    x = __uint_as_float(s[0]) + __uint_as_float(s[1]);
    auto t = __builtin_amdgcn_permlane32_swap(__float_as_uint(x), __float_as_uint(x), false, false);
    return __uint_as_float(t[0]) + __uint_as_float(t[1]);
}
__device__ __forceinline__ float wave_sum(float x) { return xrow_sum(ror_sum(quad_sum(x))); }

__device__ __forceinline__ u32 plo(u32 a, u32 b) { return __builtin_amdgcn_perm(b, a, 0x05040100u); }
__device__ __forceinline__ u32 phi(u32 a, u32 b) { return __builtin_amdgcn_perm(b, a, 0x07060302u); }

__device__ __forceinline__ f32x4 mfma16(u32x2 a, u32x2 b, f32x4 c) {
    return __builtin_amdgcn_mfma_f32_16x16x16bf16_1k(__builtin_bit_cast(s16x4, a), __builtin_bit_cast(s16x4, b), c, 0, 0, 0);
}
__device__ __forceinline__ f32x4 mfma32(u32x4 a, u32x4 b, f32x4 c) {
    return __builtin_amdgcn_mfma_f32_16x16x32_bf16(__builtin_bit_cast(bf16x8, a), __builtin_bit_cast(bf16x8, b), c, 0, 0, 0);
}
__device__ __forceinline__ float sel4(f32x4 d, int s) {
    float lo = (s & 1) ? d[1] : d[0], hi = (s & 1) ? d[3] : d[2];
    return (s & 2) ? hi : lo;
}
__device__ __forceinline__ void transpose4x8(const u32x4 (&w)[4], u32x2 (&f)[8]) {
#pragma unroll
    for (int r = 0; r < 4; ++r) {
        f[2 * r] = u32x2{plo(w[0][r], w[1][r]), plo(w[2][r], w[3][r])};
        f[2 * r + 1] = u32x2{phi(w[0][r], w[1][r]), phi(w[2][r], w[3][r])};
    }
}
struct Tag2 { u32 t1, t2; };
__device__ __forceinline__ Tag2 make_tag2(u32 tag) { return Tag2{tag & 0xffffu, ((tag * 40503u) ^ (tag >> 16)) & 0xffffu}; }
__device__ __forceinline__ u32x4 pk24(float a, float b, float c, float d, Tag2 t) {
    const u32 ua = __float_as_uint(a) + 0x80u, ub = __float_as_uint(b) + 0x80u, uc = __float_as_uint(c) + 0x80u, ud = __float_as_uint(d) + 0x80u;
    u32x4 r;
    r.x = __builtin_amdgcn_perm(ua, ub, 0x07060503u);
    r.y = (__builtin_amdgcn_perm(ub, ub, 0x02010c0cu)) | t.t1;
    r.z = __builtin_amdgcn_perm(uc, ud, 0x07060503u);
    r.w = (__builtin_amdgcn_perm(ud, ud, 0x02010c0cu)) | t.t2;
    return r;
}
__device__ __forceinline__ u32 pk24_bad(u32x4 u, Tag2 t) { return ((u.y ^ t.t1) | (u.w ^ t.t2)) & 0xffffu; }
__device__ __forceinline__ f32x4 up24(u32x4 u) {
    return f32x4{__uint_as_float(u.x & 0xffffff00u), __uint_as_float(__builtin_amdgcn_perm(u.x, u.y, 0x0403020cu)),
                 __uint_as_float(u.z & 0xffffff00u), __uint_as_float(__builtin_amdgcn_perm(u.z, u.w, 0x0403020cu))};
}
#define SC1 16
#define NT 2
#define CBAR() asm volatile("" ::: "memory")
#define KEEP4(v) asm volatile("" : "+v"((v).x), "+v"((v).y), "+v"((v).z), "+v"((v).w))
#define LDS_FENCE() asm volatile("s_waitcnt lgkmcnt(0)" ::: "memory")
#define LDS_LD(p) __hip_atomic_load((p), __ATOMIC_RELAXED, __HIP_MEMORY_SCOPE_WORKGROUP)
#define SB() __builtin_amdgcn_sched_barrier(0)

#define TS(k)
#define TS_DECL
#define TS_FLUSH()

__device__ __forceinline__ void tile_phase(unsigned char* smem, __amdgpu_buffer_rsrc_t rs, const u32x4 (&tw)[4], u32x2 af, int slot, int cidx, int goff, Tag2 tg, int lane) {
    const int g = lane >> 4, c = lane & 15;
    u32x2 f[8];
    transpose4x8(tw, f);
    const f32x4 z = {0.f, 0.f, 0.f, 0.f};
    float v[8];
#pragma unroll
    for (int e = 0; e < 8; ++e) { const f32x4 d = mfma16(af, f[e], z); const float sv = (c & 8) ? d[2] : d[0]; v[e] = sv + dppf<ROR8>(sv); }
    if (c < 8) {
        float* dst = reinterpret_cast<float*>(smem + L_TP) + (slot * 4 + g) * 64 + c * 8;
        *reinterpret_cast<f32x4*>(dst) = f32x4{v[0], v[1], v[2], v[3]};
        *reinterpret_cast<f32x4*>(dst + 4) = f32x4{v[4], v[5], v[6], v[7]};
    }
    LDS_FENCE();
    u32 old = 0;
    if (lane == 0) old = __hip_atomic_fetch_add(reinterpret_cast<u32*>(smem + L_CNT) + cidx, 1u, __ATOMIC_RELAXED, __HIP_MEMORY_SCOPE_WORKGROUP);
    old = __builtin_amdgcn_readfirstlane(old);
    if (old == 3u) {
        CBAR();
        const float* src = reinterpret_cast<const float*>(smem + L_TP) + (slot & 4) * 256 + lane * 4;
        f32x4 a = *reinterpret_cast<const f32x4*>(src);
#pragma unroll
        for (int w = 1; w < 4; ++w) a += *reinterpret_cast<const f32x4*>(src + w * 256);
        __builtin_amdgcn_raw_buffer_store_b128(pk24(a[0], a[1], a[2], a[3], tg), rs, goff + lane * 16, 0, SC1);
        SB();
        if (lane == 0) __hip_atomic_store(reinterpret_cast<u32*>(smem + L_CNT) + cidx, 5u, __ATOMIC_RELAXED, __HIP_MEMORY_SCOPE_WORKGROUP);
    }
}

__global__ void __launch_bounds__(NTHREADS, 4)
fused_decode(const u16* __restrict__ x, const u16* __restrict__ kc, const u16* __restrict__ vc,
             const u16* __restrict__ wq, const u16* __restrict__ wo, const u16* __restrict__ w1,
             const u16* __restrict__ b1, const u16* __restrict__ w2, const u16* __restrict__ b2,
             const u16* __restrict__ gw, u16* __restrict__ out, unsigned char* ws) {
    __shared__ __attribute__((aligned(16))) unsigned char smem[LDS_BYTES];
    const int tid = threadIdx.x, lane = tid & 63;
    const int wave = __builtin_amdgcn_readfirstlane(tid >> 6);
    const int bid = blockIdx.x, j = bid & 7, i = bid >> 3;
    const bool setA = i < 14, setB = (i >= 14) && (i < 28);
    const int g = lane >> 4, c = lane & 15;
    const __amdgpu_buffer_rsrc_t rs = __builtin_amdgcn_make_buffer_rsrc(ws, 0, WS_USED, 0x00020000);
    const int ab = i >> 3, as = i & 7;
    const int rg = 8 * j + (i >> 2), cgp = i & 3;
    const int tb = j * 14 + (i - 14), rg2 = tb >> 2, cg2 = tb & 3;
    u32* const cnt = reinterpret_cast<u32*>(smem + L_CNT);
    TS_DECL
    TS(0);
    const unsigned long long tstart = __builtin_amdgcn_s_memrealtime();

    if (wave < 8) {
        const bool lowh = wave < 4;
        const int w4 = wave & 3;
        const int r0 = 128 * (i >> 3) + 32 * w4;
        const __amdgpu_buffer_rsrc_t r1 = __builtin_amdgcn_make_buffer_rsrc((void*)wq, 0, 512 * 4096 * 2, 0x00020000);
        const int off1 = lowh ? (r0 + (g * 2 + (c >> 3)) * 4) * 8192 + (512 * j + 64 * (i & 7) + (c & 7) * 8) * 2 : OOB;
        u32x4 t1[4];
#pragma unroll
        for (int t = 0; t < 4; ++t) t1[t] = __builtin_amdgcn_raw_buffer_load_b128(r1, off1 + t * 8192, 0, NT);
        const u32x2 xa = *reinterpret_cast<const u32x2*>(x + (c >> 2) * 512 + r0 + (g * 2 + ((c >> 1) & 1)) * 4);
        const bool has2 = lowh || setB;
        const __amdgpu_buffer_rsrc_t r2 = __builtin_amdgcn_make_buffer_rsrc((void*)(lowh ? wo : w2), 0, lowh ? 4096 * 512 * 2 : 1792 * 512 * 2, 0x00020000);
        const int off2 = has2 ? ((64 * (lowh ? rg : rg2) + 16 * w4 + 4 * g) * 512 + 128 * (lowh ? cgp : cg2) + 8 * c) * 2 : OOB;
        u32x4 kf[4], vf[4], wf[4];
        const u32 ev = __builtin_amdgcn_raw_buffer_load_b32(rs, E_OFF, 0, SC1);
        const __amdgpu_buffer_rsrc_t rb1 = __builtin_amdgcn_make_buffer_rsrc((void*)b1, 0, 1792 * 2, 0x00020000);
        const u32x2 b1v = __builtin_amdgcn_raw_buffer_load_b64(rb1, (!lowh && setB) ? (64 * rg2 + 16 * w4 + 4 * g) * 2 : OOB, 0, 0);
        const u32 b2v = *reinterpret_cast<const u32*>(b2 + 64 * j + 2 * i);
        const __amdgpu_buffer_rsrc_t rk = __builtin_amdgcn_make_buffer_rsrc((void*)kc, 0, 32 << 20, 0x00020000);
        const __amdgpu_buffer_rsrc_t rv = __builtin_amdgcn_make_buffer_rsrc((void*)vc, 0, 32 << 20, 0x00020000);
        const int kvb = ((ab * 8 + j) * 4096 + 3072 + 128 * as + 16 * wave) * 256;
        const int offk = kvb + (4 * ((lane >> 2) & 3)) * 256 + (g * 4 + (lane & 3)) * 16;
        const int offv = kvb + (4 * g) * 256 + c * 16;
        SB();
#pragma unroll
        for (int t = 0; t < WEARLY; ++t) wf[t] = __builtin_amdgcn_raw_buffer_load_b128(r2, off2 + t * 1024, 0, NT);
        SB();
        __syncthreads();
        SB();
        const u32 tag = __builtin_amdgcn_readfirstlane(ev) + 1u;
        const Tag2 tg = make_tag2(tag);

        if (lowh) tile_phase(smem, rs, t1, xa, wave, 0, GQ_OFF + ((j * 8 + (i & 7)) * 4 + (i >> 3)) * 1024, tg, lane);
        TS(1);
        while (LDS_LD(cnt + 0) < 5u) __builtin_amdgcn_s_sleep(1);
        CBAR();
#pragma unroll
        for (int t = 0; t < 4; ++t) kf[t] = __builtin_amdgcn_raw_buffer_load_b128(rk, offk + t * 256, 0, NT);
        SB();
        SB();
        KEEP4(kf[0]); KEEP4(kf[1]);
        while (LDS_LD(cnt + 6) == 0u) __builtin_amdgcn_s_sleep(1);
        CBAR();
        vf[0] = __builtin_amdgcn_raw_buffer_load_b128(rv, offv, 0, NT); vf[1] = __builtin_amdgcn_raw_buffer_load_b128(rv, offv + 256, 0, NT);
        SB();
        KEEP4(kf[2]); KEEP4(kf[3]);
        vf[2] = __builtin_amdgcn_raw_buffer_load_b128(rv, offv + 512, 0, NT); vf[3] = __builtin_amdgcn_raw_buffer_load_b128(rv, offv + 768, 0, NT);
        SB();
        TS(2);
        TS(3);
        while (LDS_LD(cnt + 3) == 0u) __builtin_amdgcn_s_sleep(1);
        CBAR();
        TS(4);
        float sc[4];
        {
            const u32x4 qf = *reinterpret_cast<const u32x4*>(smem + L_QS + (((lane >> 2) & 3) * 128 + (g * 4 + (lane & 3)) * 8) * 2);
            const f32x4 z = {0.f, 0.f, 0.f, 0.f};
#pragma unroll
            for (int t = 0; t < 4; ++t) {
                const f32x4 d = mfma32(kf[t], qf, z);
                sc[t] = rbf(rbf(quad_sum(sel4(d, lane & 3))) * 0.08838834764831845f);
            }
        }
        u32x2 pa;
        {
            const float M = xrow_max(fmaxf(fmaxf(sc[0], sc[1]), fmaxf(sc[2], sc[3])));
            const float p0 = __builtin_amdgcn_exp2f((sc[0] - M) * LOG2E), p1 = __builtin_amdgcn_exp2f((sc[1] - M) * LOG2E);
            const float p2 = __builtin_amdgcn_exp2f((sc[2] - M) * LOG2E), p3 = __builtin_amdgcn_exp2f((sc[3] - M) * LOG2E);
            pa = u32x2{pack_bf16(p0, p1), pack_bf16(p2, p3)};
            const float l = xrow_sum((p0 + p1) + (p2 + p3));
            if (g == 0 && (c & 3) == 0) { reinterpret_cast<float*>(smem + L_MX)[(c >> 2) * 8 + wave] = M; reinterpret_cast<float*>(smem + L_LS)[(c >> 2) * 8 + wave] = l; }
        }
        TS(5);
        {
            u32x2 f[8];
            transpose4x8(vf, f);
            const f32x4 z = {0.f, 0.f, 0.f, 0.f};
            float o8[8];
#pragma unroll
            for (int e = 0; e < 8; ++e) o8[e] = mfma16(pa, f[e], z)[0];
            float* dst = reinterpret_cast<float*>(smem + L_OP) + (wave * 4 + g) * 128 + c * 8;
            *reinterpret_cast<f32x4*>(dst) = f32x4{o8[0], o8[1], o8[2], o8[3]};
            *reinterpret_cast<f32x4*>(dst + 4) = f32x4{o8[4], o8[5], o8[6], o8[7]};
        }
        __syncthreads();
        if (wave >= 4) {
            const int hl = wave - 4, pc = lane & 7;
            const float* src = reinterpret_cast<const float*>(smem + L_OP) + hl * 128 + (lane >> 3) * 16 + (pc & 3) * 4;
            const f32x4 la = *reinterpret_cast<const f32x4*>(smem + L_LS + hl * 32), lb_ = *reinterpret_cast<const f32x4*>(smem + L_LS + hl * 32 + 16);
            const f32x4 ma = *reinterpret_cast<const f32x4*>(smem + L_MX + hl * 32), mb = *reinterpret_cast<const f32x4*>(smem + L_MX + hl * 32 + 16);
            const float Mh = fmaxf(fmaxf(fmaxf(ma[0], ma[1]), fmaxf(ma[2], ma[3])), fmaxf(fmaxf(mb[0], mb[1]), fmaxf(mb[2], mb[3])));
            f32x4 a = {0.f, 0.f, 0.f, 0.f};
            float L = 0.f;
#pragma unroll
            for (int w = 0; w < 8; ++w) {
                const float sw = __builtin_amdgcn_exp2f(((w < 4 ? ma[w & 3] : mb[w & 3]) - Mh) * LOG2E);
                a += sw * *reinterpret_cast<const f32x4*>(src + w * 512);
                L = fmaf(sw, w < 4 ? la[w & 3] : lb_[w & 3], L);
            }
            const u32x4 gd = pk24(a[0], a[1], a[2], a[3], tg);
            const u32x4 gv = {pc < 4 ? gd.x : __float_as_uint(Mh), pc < 4 ? gd.y : tag, pc < 4 ? gd.z : __float_as_uint(L), pc < 4 ? gd.w : tag};
            __builtin_amdgcn_raw_buffer_store_b128(gv, rs, pc <= 4 ? GO_OFF + ((((ab * 8 + j) * 8 + as) * 4 + hl) * 64 + lane) * 16 : OOB, 0, SC1);
            if (wave == 7) TS(11);
        }
        TS(6);
        if (has2) {
            u32x2 bfr[8];
            transpose4x8(wf, bfr);
#pragma unroll
            for (int e = 0; e < 8; ++e) asm volatile("" : "+v"(bfr[e].x), "+v"(bfr[e].y));
            u32x2 af;
            const int lb = c >> 2, sq = c & 3;
            if (lowh) {
                const int head = rg >> 1, hl = head & 3;
                const int offl = GO_OFF + ((((lb * 8 + j) * 8 + sq) * 4 + hl) * 8 + 4 * (rg & 1) + w4) * 128;
                u32x4 g0 = {0u, 0u, 0u, 0u}, g1 = g0, m0 = g0, m1 = g0;
                bool ok = false;
                __builtin_amdgcn_s_sleep(H2_DELAY);
                for (unsigned spins = 0;;) {
                    CBAR();
                    if (!ok) {
                        g0 = __builtin_amdgcn_raw_buffer_load_b128(rs, offl + g * 16, 0, SC1);
                        g1 = __builtin_amdgcn_raw_buffer_load_b128(rs, offl + g * 16 + 16384, 0, SC1);
                        m0 = __builtin_amdgcn_raw_buffer_load_b128(rs, offl + 64, 0, SC1);
                        m1 = __builtin_amdgcn_raw_buffer_load_b128(rs, offl + 64 + 16384, 0, SC1);
                        ok = ((pk24_bad(g0, tg) | pk24_bad(g1, tg)) | (((m0.y ^ tag) | (m0.w ^ tag)) | ((m1.y ^ tag) | (m1.w ^ tag)))) == 0u;
                    }
                    if (__all(ok) || ++spins > SPIN_CAP) break;
                    __builtin_amdgcn_s_sleep(H2_SLEEP);
                }
                const float ma = __uint_as_float(m0.x), mb = __uint_as_float(m1.x);
                const float MM = quad_max(fmaxf(ma, mb));
                const float w0 = __builtin_amdgcn_exp2f((ma - MM) * LOG2E), w1_ = __builtin_amdgcn_exp2f((mb - MM) * LOG2E);
                const float LL = quad_sum(fmaf(w0, __uint_as_float(m0.z), w1_ * __uint_as_float(m1.z)));
                const float inv = 1.0f / LL;
                const f32x4 oa = up24(g0), ob = up24(g1);
                const float v0 = quad_sum(fmaf(w0, oa[0], w1_ * ob[0])) * inv, v1 = quad_sum(fmaf(w0, oa[1], w1_ * ob[1])) * inv;
                const float v2 = quad_sum(fmaf(w0, oa[2], w1_ * ob[2])) * inv, v3 = quad_sum(fmaf(w0, oa[3], w1_ * ob[3])) * inv;
                af = u32x2{pack_bf16(v0, v1), pack_bf16(v2, v3)};
            } else {
                const int offh = GH_OFF + ((rg2 * 4 + sq) * 4 + lb) * 256 + (8 * w4 + 2 * g) * 8;
                u32x4 gv;
                for (unsigned spins = 0;;) {
                    CBAR();
                    gv = __builtin_amdgcn_raw_buffer_load_b128(rs, offh, 0, SC1);
                    if (__all(pk24_bad(gv, tg) == 0u) || ++spins > SPIN_CAP) break;
                    __builtin_amdgcn_s_sleep(H2_SLEEP);
                }
                const f32x4 hp = up24(gv);
                float zz[4] = {quad_sum(hp[0]), quad_sum(hp[1]), quad_sum(hp[2]), quad_sum(hp[3])};
                const float bb[4] = {bf_lo(b1v.x), bf_hi(b1v.x), bf_lo(b1v.y), bf_hi(b1v.y)};
#pragma unroll
                for (int e = 0; e < 4; ++e) {
                    const float z = rbf(rbf(zz[e]) + bb[e]);
                    const float u = 0.7978845608028654f * (z + 0.044715f * z * z * z);
                    const float th = 1.0f - 2.0f / (__expf(2.0f * u) + 1.0f);
                    zz[e] = 0.5f * z * (1.0f + th);
                }
                af = u32x2{pack_bf16(zz[0], zz[1]), pack_bf16(zz[2], zz[3])};
            }
            TS(7);
            const f32x4 z = {0.f, 0.f, 0.f, 0.f};
            float o8[8];
#pragma unroll
            for (int e = 0; e < 8; ++e) o8[e] = mfma16(af, bfr[e], z)[0];
            float* dst = reinterpret_cast<float*>(smem + L_WP) + (wave * 4 + g) * 128 + c * 8;
            *reinterpret_cast<f32x4*>(dst) = f32x4{o8[0], o8[1], o8[2], o8[3]};
            *reinterpret_cast<f32x4*>(dst + 4) = f32x4{o8[4], o8[5], o8[6], o8[7]};
        }
        __syncthreads();
        if (has2 && w4 < 2) {
            const int t7 = w4 * 64 + lane, r = t7 >> 1, u2 = t7 & 1, wb = wave & 4;
            f32x2 s0 = {0.f, 0.f}, s1 = {0.f, 0.f};
#pragma unroll
            for (int w = 0; w < 4; ++w) {
                s0 += *reinterpret_cast<const f32x2*>(smem + L_WP + (((wb + w) * 4 + 2 * u2) * 128 + 2 * r) * 4);
                s1 += *reinterpret_cast<const f32x2*>(smem + L_WP + (((wb + w) * 4 + 2 * u2 + 1) * 128 + 2 * r) * 4);
            }
            const int cgx = lowh ? cgp : cg2, slot3 = lowh ? rg : 64 + rg2;
            __builtin_amdgcn_raw_buffer_store_b128(pk24(s0.x, s0.y, s1.x, s1.y, tg), rs, G3_OFF + ((cgx * 96 + slot3) * 128 + t7) * 16, 0, SC1);
        }
        TS(8);
        if (wave == FINW) {
            const int cgf = j >> 1, rr = (j & 1) * 32 + i;
            const int pp = lane >> 1, u2 = lane & 1;
            const int e = ((lane >> 5) << 1) | ((lane >> 4) & 1), ob = 2 * u2 + (e >> 1), oc = e & 1;
            const float gt = reinterpret_cast<const float*>(smem + L_ST)[ob * 4 + 3];
            const float b2f = oc ? bf_hi(b2v) : bf_lo(b2v);
            const float omg = rbf(1.0f - gt);
            const int off = G3_OFF + (((cgf * 96 + pp) * 64 + rr) * 2 + u2) * 16;
            u32x4 gv[3];
#pragma unroll
            for (int u = 0; u < 3; ++u) gv[u] = u32x4{0u, 0u, 0u, 0u};
            bool ok = false;
            __builtin_amdgcn_s_sleep(H3_DELAY);
            for (unsigned spins = 0;;) {
                CBAR();
                if (!ok) {
#pragma unroll
                    for (int u = 0; u < 3; ++u) gv[u] = __builtin_amdgcn_raw_buffer_load_b128(rs, off + u * (32 * 2048), 0, SC1);
                    u32 bad = pk24_bad(gv[0], tg) | pk24_bad(gv[1], tg);
                    if (pp < 28) bad |= pk24_bad(gv[2], tg);
                    ok = bad == 0u;
                }
                if (__all(ok) || ++spins > SPIN_CAP) break;
                __builtin_amdgcn_s_sleep(H3_SLEEP);
            }
            const f32x4 av = up24(gv[0]) + up24(gv[1]);
            f32x4 cv = up24(gv[2]);
            if (pp >= 28) cv = f32x4{0.f, 0.f, 0.f, 0.f};
            const bool isc = (lane & 2) != 0;
            float v[4];
#pragma unroll
            for (int k = 0; k < 4; ++k) { const float keep = isc ? cv[k] : av[k], give = isc ? av[k] : cv[k]; v[k] = keep + dppf<XOR2>(give); }
            auto sw0 = __builtin_amdgcn_permlane32_swap(__float_as_uint(v[0]), __float_as_uint(v[2]), false, false);
            auto sw1 = __builtin_amdgcn_permlane32_swap(__float_as_uint(v[1]), __float_as_uint(v[3]), false, false);
            const float x0 = __uint_as_float(sw0[0]) + __uint_as_float(sw0[1]), x1 = __uint_as_float(sw1[0]) + __uint_as_float(sw1[1]);
            auto sw2 = __builtin_amdgcn_permlane16_swap(__float_as_uint(x0), __float_as_uint(x1), false, false);
            float y = __uint_as_float(sw2[0]) + __uint_as_float(sw2[1]);
            y = ror_sum(y);
            const float other = dppf<XOR2>(y);
            const float res = rbf(gt * rbf(y)) + rbf(omg * rbf(rbf(other) + b2f));
            if ((lane & 14) == 0) out[ob * 512 + 64 * j + 2 * i + oc] = __builtin_bit_cast(u16, (__bf16)res);
            if (bid == 0 && lane == 0) __builtin_amdgcn_raw_buffer_store_b32(tag, rs, E_OFF, 0, SC1);
            TS(9);
        }
        TS_FLUSH();
    } else {
        const u32 ev = __builtin_amdgcn_raw_buffer_load_b32(rs, E_OFF, 0, SC1);
        if (wave > 8) {
            const int aw = wave - 9, ta = j * 14 + i, kq = ta & 3, cb = ta >> 2, r0 = 128 * kq + 32 * aw;
            const u32x4 xrow = *reinterpret_cast<const u32x4*>(x + aw * 512 + lane * 8);
            const u32x4 gwv = *reinterpret_cast<const u32x4*>(gw + lane * 8);
            const u32x2 xa = *reinterpret_cast<const u32x2*>(x + (c >> 2) * 512 + r0 + (g * 2 + ((c >> 1) & 1)) * 4);
            const __amdgpu_buffer_rsrc_t r1 = __builtin_amdgcn_make_buffer_rsrc((void*)w1, 0, 512 * 1792 * 2, 0x00020000);
            const int off1 = setA ? (r0 + (g * 2 + (c >> 3)) * 4) * 3584 + (64 * cb + (c & 7) * 8) * 2 : OOB;
            u32x4 t2[4];
#pragma unroll
            for (int t = 0; t < 4; ++t) t2[t] = __builtin_amdgcn_raw_buffer_load_b128(r1, off1 + t * 3584, 0, NT);
            SB();
            __syncthreads();
            SB();
            float f[8];
#pragma unroll
            for (int e = 0; e < 4; ++e) { f[2 * e] = bf_lo(xrow[e]); f[2 * e + 1] = bf_hi(xrow[e]); }
            const float mean = wave_sum(((f[0] + f[1]) + (f[2] + f[3])) + ((f[4] + f[5]) + (f[6] + f[7]))) * (1.0f / 512.0f);
            float vs = 0.f, gsum = 0.f;
#pragma unroll
            for (int e = 0; e < 4; ++e) {
                const float d0 = f[2 * e] - mean, d1 = f[2 * e + 1] - mean; vs = fmaf(d0, d0, vs); vs = fmaf(d1, d1, vs);
                gsum += rbf(f[2 * e] * bf_lo(gwv[e])); gsum += rbf(f[2 * e + 1] * bf_hi(gwv[e]));
            }
            const float var = wave_sum(vs) * (1.0f / 512.0f);
            const float gg = rbf(wave_sum(gsum));
            const float sd = rbf(sqrtf(rbf(rbf(var) + 1e-5f)));
            if (lane == 0) *reinterpret_cast<f32x4*>(smem + L_ST + aw * 16) = f32x4{rbf(mean), sd, 1.0f / sd, rbf(1.0f / (1.0f + __expf(-gg)))};
            LDS_FENCE();
            if (lane == 0) __hip_atomic_fetch_add(cnt + 2, 1u, __ATOMIC_RELAXED, __HIP_MEMORY_SCOPE_WORKGROUP);
            if (setA) {
                while (LDS_LD(cnt + 2) < 4u) __builtin_amdgcn_s_sleep(1);
                CBAR();
                const f32x4 stt = *reinterpret_cast<const f32x4*>(smem + L_ST + (c >> 2) * 16);
                float d[4] = {rbf(bf_lo(xa.x) - stt[0]), rbf(bf_hi(xa.x) - stt[0]), rbf(bf_lo(xa.y) - stt[0]), rbf(bf_hi(xa.y) - stt[0])};
#pragma unroll
                for (int e = 0; e < 4; ++e) { const float q0 = d[e] * stt[2]; d[e] = fmaf(fmaf(-q0, stt[1], d[e]), stt[2], q0); }
                const u32 tag = __builtin_amdgcn_readfirstlane(ev) + 1u;
                tile_phase(smem, rs, t2, u32x2{pack_bf16(d[0], d[1]), pack_bf16(d[2], d[3])}, 4 + aw, 1, GH_OFF + (cb * 4 + kq) * 1024, make_tag2(tag), lane);
            }
            return;
        }
        if (lane < 8) cnt[lane] = 0u;
        SB();
        __syncthreads();
        SB();
        TS(1);
        const u32 tag = __builtin_amdgcn_readfirstlane(ev) + 1u;
        const Tag2 tg = make_tag2(tag);
        while (LDS_LD(cnt + 0) < 4u) __builtin_amdgcn_s_sleep(2);
        while (__builtin_amdgcn_s_memrealtime() - tstart < QDELAY) __builtin_amdgcn_s_sleep(1);
        if (wave > 8) __builtin_amdgcn_s_sleep(QSLEEP * 2);
        if (wave > 9) __builtin_amdgcn_s_sleep(QSLEEP * 2);
        const int off = GQ_OFF + (((j * 8 + (lane >> 3)) * 4) * 4 + ab) * 256 + (lane & 7) * 32;
        for (unsigned spins = 0;; ++spins) {
            CBAR();
            u32x4 pa[4], pb[4];
#pragma unroll
            for (int k = 0; k < 4; ++k) { pa[k] = __builtin_amdgcn_raw_buffer_load_b128(rs, off + k * 1024, 0, SC1); pb[k] = __builtin_amdgcn_raw_buffer_load_b128(rs, off + k * 1024 + 16, 0, SC1); }
            SB();
            if (spins == 0 && lane == 0) __hip_atomic_store(cnt + 6, 1u, __ATOMIC_RELAXED, __HIP_MEMORY_SCOPE_WORKGROUP);
            SB();
            u32 bad = 0;
#pragma unroll
            for (int k = 0; k < 4; ++k) bad |= pk24_bad(pa[k], tg) | pk24_bad(pb[k], tg);
            if (__all(bad == 0u) || spins > SPIN_CAP) {
                const f32x4 qa = (up24(pa[0]) + up24(pa[1])) + (up24(pa[2]) + up24(pa[3])), qb = (up24(pb[0]) + up24(pb[1])) + (up24(pb[2]) + up24(pb[3]));
                const u32x4 qv = {pack_bf16(qa[0], qa[1]), pack_bf16(qa[2], qa[3]), pack_bf16(qb[0], qb[1]), pack_bf16(qb[2], qb[3])};
                *reinterpret_cast<u32x4*>(smem + L_QS + lane * 16) = qv;
                LDS_FENCE();
                if (lane == 0) __hip_atomic_store(cnt + 3, 1u, __ATOMIC_RELAXED, __HIP_MEMORY_SCOPE_WORKGROUP);
                break;
            }
        }
        TS(3);
        TS_FLUSH();
    }
}

extern "C" void kernel_launch(void* const* d_in, const int* in_sizes, int n_in,
                              void* d_out, int out_size, void* d_ws, size_t ws_size,
                              hipStream_t stream) {
    fused_decode<<<256, NTHREADS, 0, stream>>>((const u16*)d_in[0], (const u16*)d_in[1], (const u16*)d_in[2],
                                               (const u16*)d_in[3], (const u16*)d_in[4], (const u16*)d_in[6],
                                               (const u16*)d_in[7], (const u16*)d_in[8], (const u16*)d_in[9],
                                               (const u16*)d_in[10], (u16*)d_out, (unsigned char*)d_ws);
}
```

```cpp
#include <hip/hip_runtime.h>
#include <hip/hip_bf16.h>
#include <stdint.h>

typedef unsigned short u16;
typedef unsigned int u32;
typedef u32 u32x2 __attribute__((ext_vector_type(2)));
typedef u32 u32x4 __attribute__((ext_vector_type(4)));
typedef float f32x2 __attribute__((ext_vector_type(2)));
typedef float f32x4 __attribute__((ext_vector_type(4)));
typedef short s16x4 __attribute__((ext_vector_type(4)));
typedef __bf16 bf16x8 __attribute__((ext_vector_type(8)));
typedef __bf16 bf16x2 __attribute__((ext_vector_type(2)));

constexpr int QDELAY = 340;
constexpr int H2_DELAY = 24, H3_DELAY = 20, H2_SLEEP = 2, H3_SLEEP = 1;
constexpr int FINW = 2;
constexpr int NTHREADS = 13 * 64;

constexpr int E_OFF = 0;
constexpr int GQ_OFF = 4096;
constexpr int GH_OFF = GQ_OFF + 262144;
constexpr int GO_OFF = GH_OFF + 131072;
constexpr int G3_OFF = GO_OFF + 1048576;
constexpr int WS_USED = G3_OFF + 4 * 96 * 64 * 32;
constexpr int OOB = 0x40000000;

constexpr int L_TP = 0;
constexpr int L_ST = L_TP + 8192;
constexpr int L_CNT = L_ST + 64;
constexpr int L_QS = L_CNT + 64;
constexpr int L_MX = L_QS + 1024;
constexpr int L_LS = L_MX + 128;
constexpr int L_OP = L_LS + 128;
constexpr int L_WP = L_OP + 16384;
constexpr int L_END = L_WP + 16384;
constexpr int LDS_BYTES = 84 * 1024;
static_assert(L_END <= LDS_BYTES, "lds");

constexpr unsigned SPIN_CAP = 1u << 18;
constexpr float LOG2E = 1.4426950408889634f;

__device__ __forceinline__ float bf_lo(u32 v) { return __uint_as_float(v << 16); }
__device__ __forceinline__ float bf_hi(u32 v) { return __uint_as_float(v & 0xffff0000u); }
__device__ __forceinline__ u32 pack_bf16(float lo, float hi) {
    bf16x2 v = {(__bf16)lo, (__bf16)hi};
    return __builtin_bit_cast(u32, v);
}
__device__ __forceinline__ float rbf(float f) { return bf_lo((u32)__builtin_bit_cast(u16, (__bf16)f)); }

template <int CTRL> __device__ __forceinline__ float dppf(float x) {
    return __builtin_bit_cast(float, __builtin_amdgcn_mov_dpp(__builtin_bit_cast(int, x), CTRL, 0xf, 0xf, true));
}
constexpr int XOR1 = 0xB1, XOR2 = 0x4E, ROR4 = 0x124, ROR8 = 0x128;
__device__ __forceinline__ float quad_sum(float x) { x += dppf<XOR1>(x); x += dppf<XOR2>(x); return x; }
__device__ __forceinline__ float quad_max(float x) { x = fmaxf(x, dppf<XOR1>(x)); x = fmaxf(x, dppf<XOR2>(x)); return x; }
__device__ __forceinline__ float ror_sum(float x) { x += dppf<ROR8>(x); x += dppf<ROR4>(x); return x; }
__device__ __forceinline__ float xrow_max(float x) {
    auto s = __builtin_amdgcn_permlane16_swap(__float_as_uint(x), __float_as_uint(x), false, false);
    x = fmaxf(__uint_as_float(s[0]), __uint_as_float(s[1]));
    auto t = __builtin_amdgcn_permlane32_swap(__float_as_uint(x), __float_as_uint(x), false, false);
    return fmaxf(__uint_as_float(t[0]), __uint_as_float(t[1]));
}
__device__ __forceinline__ float xrow_sum(float x) {
    auto s = __builtin_amdgcn_permlane16_swap(__float_as_uint(x), __float_as_uint(x), false, false);
    x = __uint_as_float(s[0]) + __uint_as_float(s[1]);
    auto t = __builtin_amdgcn_permlane32_swap(__float_as_uint(x), __float_as_uint(x), false, false);
    return __uint_as_float(t[0]) + __uint_as_float(t[1]);
}
__device__ __forceinline__ float wave_sum(float x) { return xrow_sum(ror_sum(quad_sum(x))); }

__device__ __forceinline__ u32 plo(u32 a, u32 b) { return __builtin_amdgcn_perm(b, a, 0x05040100u); }
__device__ __forceinline__ u32 phi(u32 a, u32 b) { return __builtin_amdgcn_perm(b, a, 0x07060302u); }

__device__ __forceinline__ f32x4 mfma16(u32x2 a, u32x2 b, f32x4 c) {
    return __builtin_amdgcn_mfma_f32_16x16x16bf16_1k(__builtin_bit_cast(s16x4, a), __builtin_bit_cast(s16x4, b), c, 0, 0, 0);
}
__device__ __forceinline__ f32x4 mfma32(u32x4 a, u32x4 b, f32x4 c) {
    return __builtin_amdgcn_mfma_f32_16x16x32_bf16(__builtin_bit_cast(bf16x8, a), __builtin_bit_cast(bf16x8, b), c, 0, 0, 0);
}
__device__ __forceinline__ float sel4(f32x4 d, int s) {
    float lo = (s & 1) ? d[1] : d[0], hi = (s & 1) ? d[3] : d[2];
    return (s & 2) ? hi : lo;
}
__device__ __forceinline__ void transpose4x8(const u32x4 (&w)[4], u32x2 (&f)[8]) {
#pragma unroll
    for (int r = 0; r < 4; ++r) {
        f[2 * r] = u32x2{plo(w[0][r], w[1][r]), plo(w[2][r], w[3][r])};
        f[2 * r + 1] = u32x2{phi(w[0][r], w[1][r]), phi(w[2][r], w[3][r])};
    }
}
struct Tag2 { u32 t1, t2; };
__device__ __forceinline__ Tag2 make_tag2(u32 tag) { return Tag2{tag & 0xffffu, ((tag * 40503u) ^ (tag >> 16)) & 0xffffu}; }
__device__ __forceinline__ u32x4 pk24(float a, float b, float c, float d, Tag2 t) {
    const u32 ua = __float_as_uint(a) + 0x80u, ub = __float_as_uint(b) + 0x80u, uc = __float_as_uint(c) + 0x80u, ud = __float_as_uint(d) + 0x80u;
    u32x4 r;
    r.x = __builtin_amdgcn_perm(ua, ub, 0x07060503u);
    r.y = (__builtin_amdgcn_perm(ub, ub, 0x02010c0cu)) | t.t1;
    r.z = __builtin_amdgcn_perm(uc, ud, 0x07060503u);
    r.w = (__builtin_amdgcn_perm(ud, ud, 0x02010c0cu)) | t.t2;
    return r;
}
__device__ __forceinline__ u32 pk24_bad(u32x4 u, Tag2 t) { return ((u.y ^ t.t1) | (u.w ^ t.t2)) & 0xffffu; }
__device__ __forceinline__ f32x4 up24(u32x4 u) {
    return f32x4{__uint_as_float(u.x & 0xffffff00u), __uint_as_float(__builtin_amdgcn_perm(u.x, u.y, 0x0403020cu)),
                 __uint_as_float(u.z & 0xffffff00u), __uint_as_float(__builtin_amdgcn_perm(u.z, u.w, 0x0403020cu))};
}
#define SC1 16
#define NT 2
#define CBAR() asm volatile("" ::: "memory")
#define KEEP4(v) asm volatile("" : "+v"((v).x), "+v"((v).y), "+v"((v).z), "+v"((v).w))
#define LDS_FENCE() asm volatile("s_waitcnt lgkmcnt(0)" ::: "memory")
#define LDS_LD(p) __hip_atomic_load((p), __ATOMIC_RELAXED, __HIP_MEMORY_SCOPE_WORKGROUP)
#define SB() __builtin_amdgcn_sched_barrier(0)

__device__ __forceinline__ void tile_phase(unsigned char* smem, __amdgpu_buffer_rsrc_t rs, const u32x4 (&tw)[4], u32x2 af, int slot, int cidx, int goff, Tag2 tg, int lane) {
    const int g = lane >> 4, c = lane & 15;
    u32x2 f[8];
    transpose4x8(tw, f);
    const f32x4 z = {0.f, 0.f, 0.f, 0.f};
    float v[8];
#pragma unroll
    for (int e = 0; e < 8; ++e) { const f32x4 d = mfma16(af, f[e], z); const float sv = (c & 8) ? d[2] : d[0]; v[e] = sv + dppf<ROR8>(sv); }
    if (c < 8) {
        float* dst = reinterpret_cast<float*>(smem + L_TP) + (slot * 4 + g) * 64 + c * 8;
        *reinterpret_cast<f32x4*>(dst) = f32x4{v[0], v[1], v[2], v[3]};
        *reinterpret_cast<f32x4*>(dst + 4) = f32x4{v[4], v[5], v[6], v[7]};
    }
    LDS_FENCE();
    u32 old = 0;
    if (lane == 0) old = __hip_atomic_fetch_add(reinterpret_cast<u32*>(smem + L_CNT) + cidx, 1u, __ATOMIC_RELAXED, __HIP_MEMORY_SCOPE_WORKGROUP);
    old = __builtin_amdgcn_readfirstlane(old);
    if (old == 3u) {
        CBAR();
        const float* src = reinterpret_cast<const float*>(smem + L_TP) + (slot & 4) * 256 + lane * 4;
        f32x4 a = *reinterpret_cast<const f32x4*>(src);
#pragma unroll
        for (int w = 1; w < 4; ++w) a += *reinterpret_cast<const f32x4*>(src + w * 256);
        __builtin_amdgcn_raw_buffer_store_b128(pk24(a[0], a[1], a[2], a[3], tg), rs, goff + lane * 16, 0, SC1);
        SB();
        if (lane == 0) __hip_atomic_store(reinterpret_cast<u32*>(smem + L_CNT) + cidx, 5u, __ATOMIC_RELAXED, __HIP_MEMORY_SCOPE_WORKGROUP);
    }
}

__global__ void __launch_bounds__(NTHREADS, 4)
fused_decode(const u16* __restrict__ x, const u16* __restrict__ kc, const u16* __restrict__ vc,
             const u16* __restrict__ wq, const u16* __restrict__ wo, const u16* __restrict__ w1,
             const u16* __restrict__ b1, const u16* __restrict__ w2, const u16* __restrict__ b2,
             const u16* __restrict__ gw, u16* __restrict__ out, unsigned char* ws) {
    __shared__ __attribute__((aligned(16))) unsigned char smem[LDS_BYTES];
    const int tid = threadIdx.x, lane = tid & 63;
    const int wave = __builtin_amdgcn_readfirstlane(tid >> 6);
    const int bid = blockIdx.x, j = bid & 7, i = bid >> 3;
    const bool setA = i < 14, setB = (i >= 14) && (i < 28);
    const int g = lane >> 4, c = lane & 15;
    const __amdgpu_buffer_rsrc_t rs = __builtin_amdgcn_make_buffer_rsrc(ws, 0, WS_USED, 0x00020000);
    const int ab = i >> 3, as = i & 7;
    const int rg = 8 * j + (i >> 2), cgp = i & 3;
    const int tb = j * 14 + (i - 14), rg2 = tb >> 2, cg2 = tb & 3;
    u32* const cnt = reinterpret_cast<u32*>(smem + L_CNT);
    const unsigned long long tstart = __builtin_amdgcn_s_memrealtime();

    if (wave < 8) {
        const bool lowh = wave < 4;
        const int w4 = wave & 3;
        const int r0 = 128 * (i >> 3) + 32 * w4;
        const __amdgpu_buffer_rsrc_t r1 = __builtin_amdgcn_make_buffer_rsrc((void*)wq, 0, 512 * 4096 * 2, 0x00020000);
        const int off1 = lowh ? (r0 + (g * 2 + (c >> 3)) * 4) * 8192 + (512 * j + 64 * (i & 7) + (c & 7) * 8) * 2 : OOB;
        u32x4 t1[4];
#pragma unroll
        for (int t = 0; t < 4; ++t) t1[t] = __builtin_amdgcn_raw_buffer_load_b128(r1, off1 + t * 8192, 0, NT);
        const u32x2 xa = *reinterpret_cast<const u32x2*>(x + (c >> 2) * 512 + r0 + (g * 2 + ((c >> 1) & 1)) * 4);
        const bool has2 = lowh || setB;
        const __amdgpu_buffer_rsrc_t r2 = __builtin_amdgcn_make_buffer_rsrc((void*)(lowh ? wo : w2), 0, lowh ? 4096 * 512 * 2 : 1792 * 512 * 2, 0x00020000);
        const int off2 = has2 ? ((64 * (lowh ? rg : rg2) + 16 * w4 + 4 * g) * 512 + 128 * (lowh ? cgp : cg2) + 8 * c) * 2 : OOB;
        u32x4 kf[4], vf[4], wf[4];
        const u32 ev = __builtin_amdgcn_raw_buffer_load_b32(rs, E_OFF, 0, SC1);
        const __amdgpu_buffer_rsrc_t rb1 = __builtin_amdgcn_make_buffer_rsrc((void*)b1, 0, 1792 * 2, 0x00020000);
        const u32x2 b1v = __builtin_amdgcn_raw_buffer_load_b64(rb1, (!lowh && setB) ? (64 * rg2 + 16 * w4 + 4 * g) * 2 : OOB, 0, 0);
        const u32 b2v = *reinterpret_cast<const u32*>(b2 + 64 * j + 2 * i);
        const __amdgpu_buffer_rsrc_t rk = __builtin_amdgcn_make_buffer_rsrc((void*)kc, 0, 32 << 20, 0x00020000);
        const __amdgpu_buffer_rsrc_t rv = __builtin_amdgcn_make_buffer_rsrc((void*)vc, 0, 32 << 20, 0x00020000);
        const int kvb = ((ab * 8 + j) * 4096 + 3072 + 128 * as + 16 * wave) * 256;
        const int offk = kvb + (4 * ((lane >> 2) & 3)) * 256 + (g * 4 + (lane & 3)) * 16;
        const int offv = kvb + (4 * g) * 256 + c * 16;
        SB();
#pragma unroll
        for (int t = 0; t < 4; ++t) wf[t] = __builtin_amdgcn_raw_buffer_load_b128(r2, off2 + t * 1024, 0, NT);
        SB();
        __syncthreads();
        SB();
        const u32 tag = __builtin_amdgcn_readfirstlane(ev) + 1u;
        const Tag2 tg = make_tag2(tag);

        if (lowh) tile_phase(smem, rs, t1, xa, wave, 0, GQ_OFF + ((j * 8 + (i & 7)) * 4 + (i >> 3)) * 1024, tg, lane);
        while (LDS_LD(cnt + 0) < 5u) __builtin_amdgcn_s_sleep(1);
        CBAR();
#pragma unroll
        for (int t = 0; t < 4; ++t) kf[t] = __builtin_amdgcn_raw_buffer_load_b128(rk, offk + t * 256, 0, NT);
        SB();
        SB();
        KEEP4(kf[0]); KEEP4(kf[1]);
        while (LDS_LD(cnt + 6) == 0u) __builtin_amdgcn_s_sleep(1);
        CBAR();
        vf[0] = __builtin_amdgcn_raw_buffer_load_b128(rv, offv, 0, NT); vf[1] = __builtin_amdgcn_raw_buffer_load_b128(rv, offv + 256, 0, NT);
        SB();
        KEEP4(kf[2]); KEEP4(kf[3]);
        vf[2] = __builtin_amdgcn_raw_buffer_load_b128(rv, offv + 512, 0, NT); vf[3] = __builtin_amdgcn_raw_buffer_load_b128(rv, offv + 768, 0, NT);
        SB();
        while (LDS_LD(cnt + 3) == 0u) __builtin_amdgcn_s_sleep(1);
        CBAR();
        float sc[4];
        {
            const u32x4 qf = *reinterpret_cast<const u32x4*>(smem + L_QS + (((lane >> 2) & 3) * 128 + (g * 4 + (lane & 3)) * 8) * 2);
            const f32x4 z = {0.f, 0.f, 0.f, 0.f};
#pragma unroll
            for (int t = 0; t < 4; ++t) {
                const f32x4 d = mfma32(kf[t], qf, z);
                sc[t] = rbf(rbf(quad_sum(sel4(d, lane & 3))) * 0.08838834764831845f);
            }
        }
        u32x2 pa;
        {
            const float M = xrow_max(fmaxf(fmaxf(sc[0], sc[1]), fmaxf(sc[2], sc[3])));
            const float p0 = __builtin_amdgcn_exp2f((sc[0] - M) * LOG2E), p1 = __builtin_amdgcn_exp2f((sc[1] - M) * LOG2E);
            const float p2 = __builtin_amdgcn_exp2f((sc[2] - M) * LOG2E), p3 = __builtin_amdgcn_exp2f((sc[3] - M) * LOG2E);
            pa = u32x2{pack_bf16(p0, p1), pack_bf16(p2, p3)};
            const float l = xrow_sum((p0 + p1) + (p2 + p3));
            if (g == 0 && (c & 3) == 0) { reinterpret_cast<float*>(smem + L_MX)[(c >> 2) * 8 + wave] = M; reinterpret_cast<float*>(smem + L_LS)[(c >> 2) * 8 + wave] = l; }
        }
        {
            u32x2 f[8];
            transpose4x8(vf, f);
            const f32x4 z = {0.f, 0.f, 0.f, 0.f};
            float o8[8];
#pragma unroll
            for (int e = 0; e < 8; ++e) o8[e] = mfma16(pa, f[e], z)[0];
            float* dst = reinterpret_cast<float*>(smem + L_OP) + (wave * 4 + g) * 128 + c * 8;
            *reinterpret_cast<f32x4*>(dst) = f32x4{o8[0], o8[1], o8[2], o8[3]};
            *reinterpret_cast<f32x4*>(dst + 4) = f32x4{o8[4], o8[5], o8[6], o8[7]};
        }
        __syncthreads();
        if (wave >= 4) {
            const int hl = wave - 4, pc = lane & 7;
            const float* src = reinterpret_cast<const float*>(smem + L_OP) + hl * 128 + (lane >> 3) * 16 + (pc & 3) * 4;
            const f32x4 la = *reinterpret_cast<const f32x4*>(smem + L_LS + hl * 32), lb_ = *reinterpret_cast<const f32x4*>(smem + L_LS + hl * 32 + 16);
            const f32x4 ma = *reinterpret_cast<const f32x4*>(smem + L_MX + hl * 32), mb = *reinterpret_cast<const f32x4*>(smem + L_MX + hl * 32 + 16);
            const float Mh = fmaxf(fmaxf(fmaxf(ma[0], ma[1]), fmaxf(ma[2], ma[3])), fmaxf(fmaxf(mb[0], mb[1]), fmaxf(mb[2], mb[3])));
            f32x4 a = {0.f, 0.f, 0.f, 0.f};
            float L = 0.f;
#pragma unroll
            for (int w = 0; w < 8; ++w) {
                const float sw = __builtin_amdgcn_exp2f(((w < 4 ? ma[w & 3] : mb[w & 3]) - Mh) * LOG2E);
                a += sw * *reinterpret_cast<const f32x4*>(src + w * 512);
                L = fmaf(sw, w < 4 ? la[w & 3] : lb_[w & 3], L);
            }
            const u32x4 gd = pk24(a[0], a[1], a[2], a[3], tg);
            const u32x4 gv = {pc < 4 ? gd.x : __float_as_uint(Mh), pc < 4 ? gd.y : tag, pc < 4 ? gd.z : __float_as_uint(L), pc < 4 ? gd.w : tag};
            __builtin_amdgcn_raw_buffer_store_b128(gv, rs, pc <= 4 ? GO_OFF + ((((ab * 8 + j) * 8 + as) * 4 + hl) * 64 + lane) * 16 : OOB, 0, SC1);
        }
        if (has2) {
            u32x2 bfr[8];
            transpose4x8(wf, bfr);
#pragma unroll
            for (int e = 0; e < 8; ++e) asm volatile("" : "+v"(bfr[e].x), "+v"(bfr[e].y));
            u32x2 af;
            const int lb = c >> 2, sq = c & 3;
            if (lowh) {
                const int head = rg >> 1, hl = head & 3;
                const int offl = GO_OFF + ((((lb * 8 + j) * 8 + sq) * 4 + hl) * 8 + 4 * (rg & 1) + w4) * 128;
                u32x4 g0 = {0u, 0u, 0u, 0u}, g1 = g0, m0 = g0, m1 = g0;
                bool ok = false;
                __builtin_amdgcn_s_sleep(H2_DELAY);
                for (unsigned spins = 0;;) {
                    CBAR();
                    if (!ok) {
                        g0 = __builtin_amdgcn_raw_buffer_load_b128(rs, offl + g * 16, 0, SC1);
                        g1 = __builtin_amdgcn_raw_buffer_load_b128(rs, offl + g * 16 + 16384, 0, SC1);
                        m0 = __builtin_amdgcn_raw_buffer_load_b128(rs, offl + 64, 0, SC1);
                        m1 = __builtin_amdgcn_raw_buffer_load_b128(rs, offl + 64 + 16384, 0, SC1);
                        ok = ((pk24_bad(g0, tg) | pk24_bad(g1, tg)) | (((m0.y ^ tag) | (m0.w ^ tag)) | ((m1.y ^ tag) | (m1.w ^ tag)))) == 0u;
                    }
                    if (__all(ok) || ++spins > SPIN_CAP) break;
                    __builtin_amdgcn_s_sleep(H2_SLEEP);
                }
                const float ma = __uint_as_float(m0.x), mb = __uint_as_float(m1.x);
                const float MM = quad_max(fmaxf(ma, mb));
                const float w0 = __builtin_amdgcn_exp2f((ma - MM) * LOG2E), w1_ = __builtin_amdgcn_exp2f((mb - MM) * LOG2E);
                const float LL = quad_sum(fmaf(w0, __uint_as_float(m0.z), w1_ * __uint_as_float(m1.z)));
                const float inv = __builtin_amdgcn_rcpf(LL);
                const f32x4 oa = up24(g0), ob = up24(g1);
                const float v0 = quad_sum(fmaf(w0, oa[0], w1_ * ob[0])) * inv, v1 = quad_sum(fmaf(w0, oa[1], w1_ * ob[1])) * inv;
                const float v2 = quad_sum(fmaf(w0, oa[2], w1_ * ob[2])) * inv, v3 = quad_sum(fmaf(w0, oa[3], w1_ * ob[3])) * inv;
                af = u32x2{pack_bf16(v0, v1), pack_bf16(v2, v3)};
            } else {
                const int offh = GH_OFF + ((rg2 * 4 + sq) * 4 + lb) * 256 + (8 * w4 + 2 * g) * 8;
                u32x4 gv;
                for (unsigned spins = 0;;) {
                    CBAR();
                    gv = __builtin_amdgcn_raw_buffer_load_b128(rs, offh, 0, SC1);
                    if (__all(pk24_bad(gv, tg) == 0u) || ++spins > SPIN_CAP) break;
                    __builtin_amdgcn_s_sleep(H2_SLEEP);
                }
                const f32x4 hp = up24(gv);
                float zz[4] = {quad_sum(hp[0]), quad_sum(hp[1]), quad_sum(hp[2]), quad_sum(hp[3])};
                const float bb[4] = {bf_lo(b1v.x), bf_hi(b1v.x), bf_lo(b1v.y), bf_hi(b1v.y)};
#pragma unroll
                for (int e = 0; e < 4; ++e) {
                    const float z = rbf(rbf(zz[e]) + bb[e]);
                    const float u = 0.7978845608028654f * (z + 0.044715f * z * z * z);
                    const float th = 1.0f - 2.0f * __builtin_amdgcn_rcpf(__expf(2.0f * u) + 1.0f);
                    zz[e] = 0.5f * z * (1.0f + th);
                }
                af = u32x2{pack_bf16(zz[0], zz[1]), pack_bf16(zz[2], zz[3])};
            }
            const f32x4 z = {0.f, 0.f, 0.f, 0.f};
            float o8[8];
#pragma unroll
            for (int e = 0; e < 8; ++e) o8[e] = mfma16(af, bfr[e], z)[0];
            float* dst = reinterpret_cast<float*>(smem + L_WP) + (wave * 4 + g) * 128 + c * 8;
            *reinterpret_cast<f32x4*>(dst) = f32x4{o8[0], o8[1], o8[2], o8[3]};
            *reinterpret_cast<f32x4*>(dst + 4) = f32x4{o8[4], o8[5], o8[6], o8[7]};
        }
        __syncthreads();
        if (has2 && w4 < 2) {
            const int t7 = w4 * 64 + lane, r = t7 >> 1, u2 = t7 & 1, wb = wave & 4;
            f32x2 s0 = {0.f, 0.f}, s1 = {0.f, 0.f};
#pragma unroll
            for (int w = 0; w < 4; ++w) {
                s0 += *reinterpret_cast<const f32x2*>(smem + L_WP + (((wb + w) * 4 + 2 * u2) * 128 + 2 * r) * 4);
                s1 += *reinterpret_cast<const f32x2*>(smem + L_WP + (((wb + w) * 4 + 2 * u2 + 1) * 128 + 2 * r) * 4);
            }
            const int cgx = lowh ? cgp : cg2, slot3 = lowh ? rg : 64 + rg2;
            __builtin_amdgcn_raw_buffer_store_b128(pk24(s0.x, s0.y, s1.x, s1.y, tg), rs, G3_OFF + ((cgx * 96 + slot3) * 128 + t7) * 16, 0, SC1);
        }
        if (wave == FINW) {
            const int cgf = j >> 1, rr = (j & 1) * 32 + i;
            const int pp = lane >> 1, u2 = lane & 1;
            const int e = ((lane >> 5) << 1) | ((lane >> 4) & 1), ob = 2 * u2 + (e >> 1), oc = e & 1;
            const float gt = reinterpret_cast<const float*>(smem + L_ST)[ob * 4 + 3];
            const float b2f = oc ? bf_hi(b2v) : bf_lo(b2v);
            const float omg = rbf(1.0f - gt);
            const int off = G3_OFF + (((cgf * 96 + pp) * 64 + rr) * 2 + u2) * 16;
            u32x4 gv[3];
#pragma unroll
            for (int u = 0; u < 3; ++u) gv[u] = u32x4{0u, 0u, 0u, 0u};
            bool ok = false;
            __builtin_amdgcn_s_sleep(H3_DELAY);
            for (unsigned spins = 0;;) {
                CBAR();
                if (!ok) {
#pragma unroll
                    for (int u = 0; u < 3; ++u) gv[u] = __builtin_amdgcn_raw_buffer_load_b128(rs, off + u * (32 * 2048), 0, SC1);
                    u32 bad = pk24_bad(gv[0], tg) | pk24_bad(gv[1], tg);
                    if (pp < 28) bad |= pk24_bad(gv[2], tg);
                    ok = bad == 0u;
                }
                if (__all(ok) || ++spins > SPIN_CAP) break;
                __builtin_amdgcn_s_sleep(H3_SLEEP);
            }
            const f32x4 av = up24(gv[0]) + up24(gv[1]);
            f32x4 cv = up24(gv[2]);
            if (pp >= 28) cv = f32x4{0.f, 0.f, 0.f, 0.f};
            const bool isc = (lane & 2) != 0;
            float v[4];
#pragma unroll
            for (int k = 0; k < 4; ++k) { const float keep = isc ? cv[k] : av[k], give = isc ? av[k] : cv[k]; v[k] = keep + dppf<XOR2>(give); }
            auto sw0 = __builtin_amdgcn_permlane32_swap(__float_as_uint(v[0]), __float_as_uint(v[2]), false, false);
            auto sw1 = __builtin_amdgcn_permlane32_swap(__float_as_uint(v[1]), __float_as_uint(v[3]), false, false);
            const float x0 = __uint_as_float(sw0[0]) + __uint_as_float(sw0[1]), x1 = __uint_as_float(sw1[0]) + __uint_as_float(sw1[1]);
            auto sw2 = __builtin_amdgcn_permlane16_swap(__float_as_uint(x0), __float_as_uint(x1), false, false);
            float y = __uint_as_float(sw2[0]) + __uint_as_float(sw2[1]);
            y = ror_sum(y);
            const float other = dppf<XOR2>(y);
            const float res = rbf(gt * rbf(y)) + rbf(omg * rbf(rbf(other) + b2f));
            if ((lane & 14) == 0) out[ob * 512 + 64 * j + 2 * i + oc] = __builtin_bit_cast(u16, (__bf16)res);
            if (bid == 0 && lane == 0) __builtin_amdgcn_raw_buffer_store_b32(tag, rs, E_OFF, 0, SC1);
        }
    } else {
        const u32 ev = __builtin_amdgcn_raw_buffer_load_b32(rs, E_OFF, 0, SC1);
        if (wave > 8) {
            const int aw = wave - 9, ta = j * 14 + i, kq = ta & 3, cb = ta >> 2, r0 = 128 * kq + 32 * aw;
            const u32x4 xrow = *reinterpret_cast<const u32x4*>(x + aw * 512 + lane * 8);
            const u32x4 gwv = *reinterpret_cast<const u32x4*>(gw + lane * 8);
            const u32x2 xa = *reinterpret_cast<const u32x2*>(x + (c >> 2) * 512 + r0 + (g * 2 + ((c >> 1) & 1)) * 4);
            const __amdgpu_buffer_rsrc_t r1 = __builtin_amdgcn_make_buffer_rsrc((void*)w1, 0, 512 * 1792 * 2, 0x00020000);
            const int off1 = setA ? (r0 + (g * 2 + (c >> 3)) * 4) * 3584 + (64 * cb + (c & 7) * 8) * 2 : OOB;
            u32x4 t2[4];
#pragma unroll
            for (int t = 0; t < 4; ++t) t2[t] = __builtin_amdgcn_raw_buffer_load_b128(r1, off1 + t * 3584, 0, NT);
            SB();
            __syncthreads();
            SB();
            float f[8];
#pragma unroll
            for (int e = 0; e < 4; ++e) { f[2 * e] = bf_lo(xrow[e]); f[2 * e + 1] = bf_hi(xrow[e]); }
            const float mean = wave_sum(((f[0] + f[1]) + (f[2] + f[3])) + ((f[4] + f[5]) + (f[6] + f[7]))) * (1.0f / 512.0f);
            float vs = 0.f, gsum = 0.f;
#pragma unroll
            for (int e = 0; e < 4; ++e) {
                const float d0 = f[2 * e] - mean, d1 = f[2 * e + 1] - mean; vs = fmaf(d0, d0, vs); vs = fmaf(d1, d1, vs);
                gsum += rbf(f[2 * e] * bf_lo(gwv[e])); gsum += rbf(f[2 * e + 1] * bf_hi(gwv[e]));
            }
            const float var = wave_sum(vs) * (1.0f / 512.0f);
            const float gg = rbf(wave_sum(gsum));
            const float sd = rbf(sqrtf(rbf(rbf(var) + 1e-5f)));
            if (lane == 0) *reinterpret_cast<f32x4*>(smem + L_ST + aw * 16) = f32x4{rbf(mean), sd, 1.0f / sd, rbf(1.0f / (1.0f + __expf(-gg)))};
            LDS_FENCE();
            if (lane == 0) __hip_atomic_fetch_add(cnt + 2, 1u, __ATOMIC_RELAXED, __HIP_MEMORY_SCOPE_WORKGROUP);
            if (setA) {
                while (LDS_LD(cnt + 2) < 4u) __builtin_amdgcn_s_sleep(1);
                CBAR();
                const f32x4 stt = *reinterpret_cast<const f32x4*>(smem + L_ST + (c >> 2) * 16);
                float d[4] = {rbf(bf_lo(xa.x) - stt[0]), rbf(bf_hi(xa.x) - stt[0]), rbf(bf_lo(xa.y) - stt[0]), rbf(bf_hi(xa.y) - stt[0])};
#pragma unroll
                for (int e = 0; e < 4; ++e) { const float q0 = d[e] * stt[2]; d[e] = fmaf(fmaf(-q0, stt[1], d[e]), stt[2], q0); }
                const u32 tag = __builtin_amdgcn_readfirstlane(ev) + 1u;
                tile_phase(smem, rs, t2, u32x2{pack_bf16(d[0], d[1]), pack_bf16(d[2], d[3])}, 4 + aw, 1, GH_OFF + (cb * 4 + kq) * 1024, make_tag2(tag), lane);
            }
            return;
        }
        u32 tch = __builtin_amdgcn_raw_buffer_load_b32(rs, lane * 65536 + 4096, 0, NT);
        tch ^= __builtin_amdgcn_raw_buffer_load_b32(__builtin_amdgcn_make_buffer_rsrc((void*)out, 0, 4096, 0x00020000), lane == 0 ? 128 * j : OOB, 0, NT);
        if (lane < 8) cnt[lane] = 0u;
        SB();
        __syncthreads();
        SB();
        asm volatile("" :: "v"(tch));
        const u32 tag = __builtin_amdgcn_readfirstlane(ev) + 1u;
        const Tag2 tg = make_tag2(tag);
        while (LDS_LD(cnt + 0) < 4u) __builtin_amdgcn_s_sleep(2);
        while (__builtin_amdgcn_s_memrealtime() - tstart < QDELAY) __builtin_amdgcn_s_sleep(1);
        const int off = GQ_OFF + (((j * 8 + (lane >> 3)) * 4) * 4 + ab) * 256 + (lane & 7) * 32;
        for (unsigned spins = 0;; ++spins) {
            CBAR();
            u32x4 pa[4], pb[4];
#pragma unroll
            for (int k = 0; k < 4; ++k) { pa[k] = __builtin_amdgcn_raw_buffer_load_b128(rs, off + k * 1024, 0, SC1); pb[k] = __builtin_amdgcn_raw_buffer_load_b128(rs, off + k * 1024 + 16, 0, SC1); }
            SB();
            if (spins == 0 && lane == 0) __hip_atomic_store(cnt + 6, 1u, __ATOMIC_RELAXED, __HIP_MEMORY_SCOPE_WORKGROUP);
            SB();
            u32 bad = 0;
#pragma unroll
            for (int k = 0; k < 4; ++k) bad |= pk24_bad(pa[k], tg) | pk24_bad(pb[k], tg);
            if (__all(bad == 0u) || spins > SPIN_CAP) {
                const f32x4 qa = (up24(pa[0]) + up24(pa[1])) + (up24(pa[2]) + up24(pa[3])), qb = (up24(pb[0]) + up24(pb[1])) + (up24(pb[2]) + up24(pb[3]));
                const u32x4 qv = {pack_bf16(qa[0], qa[1]), pack_bf16(qa[2], qa[3]), pack_bf16(qb[0], qb[1]), pack_bf16(qb[2], qb[3])};
                *reinterpret_cast<u32x4*>(smem + L_QS + lane * 16) = qv;
                LDS_FENCE();
                if (lane == 0) __hip_atomic_store(cnt + 3, 1u, __ATOMIC_RELAXED, __HIP_MEMORY_SCOPE_WORKGROUP);
                break;
            }
        }
    }
}

extern "C" void kernel_launch(void* const* d_in, const int* in_sizes, int n_in,
                              void* d_out, int out_size, void* d_ws, size_t ws_size,
                              hipStream_t stream) {
    fused_decode<<<256, NTHREADS, 0, stream>>>((const u16*)d_in[0], (const u16*)d_in[1], (const u16*)d_in[2],
                                               (const u16*)d_in[3], (const u16*)d_in[4], (const u16*)d_in[6],
                                               (const u16*)d_in[7], (const u16*)d_in[8], (const u16*)d_in[9],
                                               (const u16*)d_in[10], (u16*)d_out, (unsigned char*)d_ws);
}
```

```cpp
#include <hip/hip_runtime.h>
#include <hip/hip_bf16.h>
#include <stdint.h>

typedef unsigned short u16;
typedef unsigned int u32;
typedef u32 u32x2 __attribute__((ext_vector_type(2)));
typedef u32 u32x4 __attribute__((ext_vector_type(4)));
typedef float f32x2 __attribute__((ext_vector_type(2)));
typedef float f32x4 __attribute__((ext_vector_type(4)));
typedef short s16x4 __attribute__((ext_vector_type(4)));
typedef __bf16 bf16x8 __attribute__((ext_vector_type(8)));
typedef __bf16 bf16x2 __attribute__((ext_vector_type(2)));

#define NPOLL 1
#define WEARLY 4
#define QPRIO 0
#define VHOLD 1
#define FINW 2
#define KTRIG 1
#define VSPLIT 1
#define QDELAY 400
#define VDELAY 0
#define QSLEEP 4
#define SCHED 1
#define H2_DELAY 24
#define H3_DELAY 20
#define H2_SLEEP 2
#define H3_SLEEP 1
#define H2_DIRECT 16
constexpr int NTHREADS = 13 * 64;

constexpr int E_OFF = 0;
constexpr int GQ_OFF = 4096;
constexpr int GH_OFF = GQ_OFF + 262144;
constexpr int GO_OFF = GH_OFF + 131072;
constexpr int G3_OFF = GO_OFF + 1048576;
constexpr int WS_USED = G3_OFF + 4 * 96 * 64 * 32;
constexpr int OOB = 0x40000000;

constexpr int L_TP = 0;
constexpr int L_ST = L_TP + 8192;
constexpr int L_CNT = L_ST + 64;
constexpr int L_QS = L_CNT + 64;
constexpr int L_MX = L_QS + 1024;
constexpr int L_LS = L_MX + 128;
constexpr int L_OP = L_LS + 128;
constexpr int L_WP = L_OP + 16384;
constexpr int L_END = L_WP + 16384;
constexpr int LDS_BYTES = 84 * 1024;
static_assert(L_END <= LDS_BYTES, "lds");

constexpr unsigned SPIN_CAP = 1u << 18;
constexpr float LOG2E = 1.4426950408889634f;

__device__ __forceinline__ float bf_lo(u32 v) { return __uint_as_float(v << 16); }
__device__ __forceinline__ float bf_hi(u32 v) { return __uint_as_float(v & 0xffff0000u); }
__device__ __forceinline__ u32 pack_bf16(float lo, float hi) {
    bf16x2 v = {(__bf16)lo, (__bf16)hi};
    return __builtin_bit_cast(u32, v);
}
__device__ __forceinline__ float rbf(float f) { return bf_lo((u32)__builtin_bit_cast(u16, (__bf16)f)); }

template <int CTRL> __device__ __forceinline__ float dppf(float x) {
    return __builtin_bit_cast(float, __builtin_amdgcn_mov_dpp(__builtin_bit_cast(int, x), CTRL, 0xf, 0xf, true));
}
constexpr int XOR1 = 0xB1, XOR2 = 0x4E, ROR4 = 0x124, ROR8 = 0x128;
__device__ __forceinline__ float quad_sum(float x) { x += dppf<XOR1>(x); x += dppf<XOR2>(x); return x; }
__device__ __forceinline__ float quad_max(float x) { x = fmaxf(x, dppf<XOR1>(x)); x = fmaxf(x, dppf<XOR2>(x)); return x; }
__device__ __forceinline__ float ror_sum(float x) { x += dppf<ROR8>(x); x += dppf<ROR4>(x); return x; }
__device__ __forceinline__ float xrow_max(float x) {
    auto s = __builtin_amdgcn_permlane16_swap(__float_as_uint(x), __float_as_uint(x), false, false);
    x = fmaxf(__uint_as_float(s[0]), __uint_as_float(s[1]));
    auto t = __builtin_amdgcn_permlane32_swap(__float_as_uint(x), __float_as_uint(x), false, false);
    return fmaxf(__uint_as_float(t[0]), __uint_as_float(t[1]));
}
__device__ __forceinline__ float xrow_sum(float x) {
    auto s = __builtin_amdgcn_permlane16_swap(__float_as_uint(x), __float_as_uint(x), false, false);
    x = __uint_as_float(s[0]) + __uint_as_float(s[1]);
    auto t = __builtin_amdgcn_permlane32_swap(__float_as_uint(x), __float_as_uint(x), false, false);
    return __uint_as_float(t[0]) + __uint_as_float(t[1]);
}
__device__ __forceinline__ float wave_sum(float x) { return xrow_sum(ror_sum(quad_sum(x))); }

__device__ __forceinline__ u32 plo(u32 a, u32 b) { return __builtin_amdgcn_perm(b, a, 0x05040100u); }
__device__ __forceinline__ u32 phi(u32 a, u32 b) { return __builtin_amdgcn_perm(b, a, 0x07060302u); }

__device__ __forceinline__ f32x4 mfma16(u32x2 a, u32x2 b, f32x4 c) {
    return __builtin_amdgcn_mfma_f32_16x16x16bf16_1k(__builtin_bit_cast(s16x4, a), __builtin_bit_cast(s16x4, b), c, 0, 0, 0);
}
__device__ __forceinline__ f32x4 mfma32(u32x4 a, u32x4 b, f32x4 c) {
    return __builtin_amdgcn_mfma_f32_16x16x32_bf16(__builtin_bit_cast(bf16x8, a), __builtin_bit_cast(bf16x8, b), c, 0, 0, 0);
}
__device__ __forceinline__ float sel4(f32x4 d, int s) {
    float lo = (s & 1) ? d[1] : d[0], hi = (s & 1) ? d[3] : d[2];
    return (s & 2) ? hi : lo;
}
__device__ __forceinline__ void transpose4x8(const u32x4 (&w)[4], u32x2 (&f)[8]) {
#pragma unroll
    for (int r = 0; r < 4; ++r) {
        f[2 * r] = u32x2{plo(w[0][r], w[1][r]), plo(w[2][r], w[3][r])};
        f[2 * r + 1] = u32x2{phi(w[0][r], w[1][r]), phi(w[2][r], w[3][r])};
    }
}
struct Tag2 { u32 t1, t2; };
__device__ __forceinline__ Tag2 make_tag2(u32 tag) { return Tag2{tag & 0xffffu, ((tag * 40503u) ^ (tag >> 16)) & 0xffffu}; }
__device__ __forceinline__ u32x4 pk24(float a, float b, float c, float d, Tag2 t) {
    const u32 ua = __float_as_uint(a) + 0x80u, ub = __float_as_uint(b) + 0x80u, uc = __float_as_uint(c) + 0x80u, ud = __float_as_uint(d) + 0x80u;
    u32x4 r;
    r.x = __builtin_amdgcn_perm(ua, ub, 0x07060503u);
    r.y = (__builtin_amdgcn_perm(ub, ub, 0x02010c0cu)) | t.t1;
    r.z = __builtin_amdgcn_perm(uc, ud, 0x07060503u);
    r.w = (__builtin_amdgcn_perm(ud, ud, 0x02010c0cu)) | t.t2;
    return r;
}
__device__ __forceinline__ u32 pk24_bad(u32x4 u, Tag2 t) { return ((u.y ^ t.t1) | (u.w ^ t.t2)) & 0xffffu; }
__device__ __forceinline__ f32x4 up24(u32x4 u) {
    return f32x4{__uint_as_float(u.x & 0xffffff00u), __uint_as_float(__builtin_amdgcn_perm(u.x, u.y, 0x0403020cu)),
                 __uint_as_float(u.z & 0xffffff00u), __uint_as_float(__builtin_amdgcn_perm(u.z, u.w, 0x0403020cu))};
}
#define SC1 16
#define NT 2
#define CBAR() asm volatile("" ::: "memory")
#define KEEP4(v) asm volatile("" : "+v"((v).x), "+v"((v).y), "+v"((v).z), "+v"((v).w))
#define LDS_FENCE() asm volatile("s_waitcnt lgkmcnt(0)" ::: "memory")
#define LDS_LD(p) __hip_atomic_load((p), __ATOMIC_RELAXED, __HIP_MEMORY_SCOPE_WORKGROUP)
#define SB() __builtin_amdgcn_sched_barrier(0)

#define TS(k)
#define TS_DECL
#define TS_FLUSH()

__device__ __forceinline__ void tile_phase(unsigned char* smem, __amdgpu_buffer_rsrc_t rs, const u32x4 (&tw)[4], u32x2 af, int slot, int cidx, int goff, Tag2 tg, int lane) {
    const int g = lane >> 4, c = lane & 15;
    u32x2 f[8];
    transpose4x8(tw, f);
    const f32x4 z = {0.f, 0.f, 0.f, 0.f};
    float v[8];
#pragma unroll
    for (int e = 0; e < 8; ++e) { const f32x4 d = mfma16(af, f[e], z); const float sv = (c & 8) ? d[2] : d[0]; v[e] = sv + dppf<ROR8>(sv); }
    if (c < 8) {
        float* dst = reinterpret_cast<float*>(smem + L_TP) + (slot * 4 + g) * 64 + c * 8;
        *reinterpret_cast<f32x4*>(dst) = f32x4{v[0], v[1], v[2], v[3]};
        *reinterpret_cast<f32x4*>(dst + 4) = f32x4{v[4], v[5], v[6], v[7]};
    }
    LDS_FENCE();
    u32 old = 0;
    if (lane == 0) old = __hip_atomic_fetch_add(reinterpret_cast<u32*>(smem + L_CNT) + cidx, 1u, __ATOMIC_RELAXED, __HIP_MEMORY_SCOPE_WORKGROUP);
    old = __builtin_amdgcn_readfirstlane(old);
    if (old == 3u) {
        CBAR();
        const float* src = reinterpret_cast<const float*>(smem + L_TP) + (slot & 4) * 256 + lane * 4;
        f32x4 a = *reinterpret_cast<const f32x4*>(src);
#pragma unroll
        for (int w = 1; w < 4; ++w) a += *reinterpret_cast<const f32x4*>(src + w * 256);
        __builtin_amdgcn_raw_buffer_store_b128(pk24(a[0], a[1], a[2], a[3], tg), rs, goff + lane * 16, 0, SC1);
        SB();
        if (lane == 0) __hip_atomic_store(reinterpret_cast<u32*>(smem + L_CNT) + cidx, 5u, __ATOMIC_RELAXED, __HIP_MEMORY_SCOPE_WORKGROUP);
    }
}

__global__ void __launch_bounds__(NTHREADS, 4)
fused_decode(const u16* __restrict__ x, const u16* __restrict__ kc, const u16* __restrict__ vc,
             const u16* __restrict__ wq, const u16* __restrict__ wo, const u16* __restrict__ w1,
             const u16* __restrict__ b1, const u16* __restrict__ w2, const u16* __restrict__ b2,
             const u16* __restrict__ gw, u16* __restrict__ out, unsigned char* ws) {
    __shared__ __attribute__((aligned(16))) unsigned char smem[LDS_BYTES];
    const int tid = threadIdx.x, lane = tid & 63;
    const int wave = __builtin_amdgcn_readfirstlane(tid >> 6);
    const int bid = blockIdx.x, j = bid & 7, i = bid >> 3;
    const bool setA = i < 14, setB = (i >= 14) && (i < 28);
    const int g = lane >> 4, c = lane & 15;
    const __amdgpu_buffer_rsrc_t rs = __builtin_amdgcn_make_buffer_rsrc(ws, 0, WS_USED, 0x00020000);
    const int ab = i >> 3, as = i & 7;
    const int rg = 8 * j + (i >> 2), cgp = i & 3;
    const int tb = j * 14 + (i - 14), rg2 = tb >> 2, cg2 = tb & 3;
    u32* const cnt = reinterpret_cast<u32*>(smem + L_CNT);
    TS_DECL
    TS(0);
    const unsigned long long tstart = __builtin_amdgcn_s_memrealtime();

    if (wave < 8) {
        const bool lowh = wave < 4;
        const int w4 = wave & 3;
        const int r0 = 128 * (i >> 3) + 32 * w4;
        const __amdgpu_buffer_rsrc_t r1 = __builtin_amdgcn_make_buffer_rsrc((void*)wq, 0, 512 * 4096 * 2, 0x00020000);
        const int off1 = lowh ? (r0 + (g * 2 + (c >> 3)) * 4) * 8192 + (512 * j + 64 * (i & 7) + (c & 7) * 8) * 2 : OOB;
        u32x4 t1[4];
#pragma unroll
        for (int t = 0; t < 4; ++t) t1[t] = __builtin_amdgcn_raw_buffer_load_b128(r1, off1 + t * 8192, 0, NT);
        const u32x2 xa = *reinterpret_cast<const u32x2*>(x + (c >> 2) * 512 + r0 + (g * 2 + ((c >> 1) & 1)) * 4);
        const bool has2 = lowh || setB;
        const __amdgpu_buffer_rsrc_t r2 = __builtin_amdgcn_make_buffer_rsrc((void*)(lowh ? wo : w2), 0, lowh ? 4096 * 512 * 2 : 1792 * 512 * 2, 0x00020000);
        const int off2 = has2 ? ((64 * (lowh ? rg : rg2) + 16 * w4 + 4 * g) * 512 + 128 * (lowh ? cgp : cg2) + 8 * c) * 2 : OOB;
        u32x4 kf[4], vf[4], wf[4];
        const u32 ev = __builtin_amdgcn_raw_buffer_load_b32(rs, E_OFF, 0, SC1);
        const __amdgpu_buffer_rsrc_t rb1 = __builtin_amdgcn_make_buffer_rsrc((void*)b1, 0, 1792 * 2, 0x00020000);
        const u32x2 b1v = __builtin_amdgcn_raw_buffer_load_b64(rb1, (!lowh && setB) ? (64 * rg2 + 16 * w4 + 4 * g) * 2 : OOB, 0, 0);
        const u32 b2v = *reinterpret_cast<const u32*>(b2 + 64 * j + 2 * i);
        const __amdgpu_buffer_rsrc_t rk = __builtin_amdgcn_make_buffer_rsrc((void*)kc, 0, 32 << 20, 0x00020000);
        const __amdgpu_buffer_rsrc_t rv = __builtin_amdgcn_make_buffer_rsrc((void*)vc, 0, 32 << 20, 0x00020000);
        const int kvb = ((ab * 8 + j) * 4096 + 3072 + 128 * as + 16 * wave) * 256;
        const int offk = kvb + (4 * ((lane >> 2) & 3)) * 256 + (g * 4 + (lane & 3)) * 16;
        const int offv = kvb + (4 * g) * 256 + c * 16;
        SB();
#pragma unroll
        for (int t = 0; t < WEARLY; ++t) wf[t] = __builtin_amdgcn_raw_buffer_load_b128(r2, off2 + t * 1024, 0, NT);
        SB();
        __syncthreads();
        SB();
        const u32 tag = __builtin_amdgcn_readfirstlane(ev) + 1u;
        const Tag2 tg = make_tag2(tag);

        if (lowh) tile_phase(smem, rs, t1, xa, wave, 0, GQ_OFF + ((j * 8 + (i & 7)) * 4 + (i >> 3)) * 1024, tg, lane);
        TS(1);
        while (LDS_LD(cnt + 0) < 5u) __builtin_amdgcn_s_sleep(1);
        CBAR();
#pragma unroll
        for (int t = 0; t < 4; ++t) kf[t] = __builtin_amdgcn_raw_buffer_load_b128(rk, offk + t * 256, 0, NT);
        SB();
        SB();
        KEEP4(kf[0]); KEEP4(kf[1]);
        if (lane == 0) __hip_atomic_fetch_add(cnt + 5, 1u, __ATOMIC_RELAXED, __HIP_MEMORY_SCOPE_WORKGROUP);
        while (LDS_LD(cnt + 6) == 0u) __builtin_amdgcn_s_sleep(1);
        CBAR();
        vf[0] = __builtin_amdgcn_raw_buffer_load_b128(rv, offv, 0, NT); vf[1] = __builtin_amdgcn_raw_buffer_load_b128(rv, offv + 256, 0, NT);
        SB();
        KEEP4(kf[2]); KEEP4(kf[3]);
        while (LDS_LD(cnt + 6) < 2u) __builtin_amdgcn_s_sleep(1);
        CBAR();
        vf[2] = __builtin_amdgcn_raw_buffer_load_b128(rv, offv + 512, 0, NT); vf[3] = __builtin_amdgcn_raw_buffer_load_b128(rv, offv + 768, 0, NT);
        SB();
        TS(2);
        TS(3);
        while (LDS_LD(cnt + 3) == 0u) __builtin_amdgcn_s_sleep(1);
        CBAR();
        TS(4);
        float sc[4];
        {
            const u32x4 qf = *reinterpret_cast<const u32x4*>(smem + L_QS + (((lane >> 2) & 3) * 128 + (g * 4 + (lane & 3)) * 8) * 2);
            const f32x4 z = {0.f, 0.f, 0.f, 0.f};
#pragma unroll
            for (int t = 0; t < 4; ++t) {
                const f32x4 d = mfma32(kf[t], qf, z);
                sc[t] = rbf(rbf(quad_sum(sel4(d, lane & 3))) * 0.08838834764831845f);
            }
        }
        u32x2 pa;
        {
            const float M = xrow_max(fmaxf(fmaxf(sc[0], sc[1]), fmaxf(sc[2], sc[3])));
            const float p0 = __builtin_amdgcn_exp2f((sc[0] - M) * LOG2E), p1 = __builtin_amdgcn_exp2f((sc[1] - M) * LOG2E);
            const float p2 = __builtin_amdgcn_exp2f((sc[2] - M) * LOG2E), p3 = __builtin_amdgcn_exp2f((sc[3] - M) * LOG2E);
            pa = u32x2{pack_bf16(p0, p1), pack_bf16(p2, p3)};
            const float l = xrow_sum((p0 + p1) + (p2 + p3));
            if (g == 0 && (c & 3) == 0) { reinterpret_cast<float*>(smem + L_MX)[(c >> 2) * 8 + wave] = M; reinterpret_cast<float*>(smem + L_LS)[(c >> 2) * 8 + wave] = l; }
        }
        TS(5);
        {
            u32x2 f[8];
            transpose4x8(vf, f);
            const f32x4 z = {0.f, 0.f, 0.f, 0.f};
            float o8[8];
#pragma unroll
            for (int e = 0; e < 8; ++e) o8[e] = mfma16(pa, f[e], z)[0];
            float* dst = reinterpret_cast<float*>(smem + L_OP) + (wave * 4 + g) * 128 + c * 8;
            *reinterpret_cast<f32x4*>(dst) = f32x4{o8[0], o8[1], o8[2], o8[3]};
            *reinterpret_cast<f32x4*>(dst + 4) = f32x4{o8[4], o8[5], o8[6], o8[7]};
        }
        __syncthreads();
        if (wave >= 4) {
            const int hl = wave - 4, pc = lane & 7;
            const float* src = reinterpret_cast<const float*>(smem + L_OP) + hl * 128 + (lane >> 3) * 16 + (pc & 3) * 4;
            const f32x4 la = *reinterpret_cast<const f32x4*>(smem + L_LS + hl * 32), lb_ = *reinterpret_cast<const f32x4*>(smem + L_LS + hl * 32 + 16);
            const f32x4 ma = *reinterpret_cast<const f32x4*>(smem + L_MX + hl * 32), mb = *reinterpret_cast<const f32x4*>(smem + L_MX + hl * 32 + 16);
            const float Mh = fmaxf(fmaxf(fmaxf(ma[0], ma[1]), fmaxf(ma[2], ma[3])), fmaxf(fmaxf(mb[0], mb[1]), fmaxf(mb[2], mb[3])));
            f32x4 a = {0.f, 0.f, 0.f, 0.f};
            float L = 0.f;
#pragma unroll
            for (int w = 0; w < 8; ++w) {
                const float sw = __builtin_amdgcn_exp2f(((w < 4 ? ma[w & 3] : mb[w & 3]) - Mh) * LOG2E);
                a += sw * *reinterpret_cast<const f32x4*>(src + w * 512);
                L = fmaf(sw, w < 4 ? la[w & 3] : lb_[w & 3], L);
            }
            const u32x4 gd = pk24(a[0], a[1], a[2], a[3], tg);
            const u32x4 gv = {pc < 4 ? gd.x : __float_as_uint(Mh), pc < 4 ? gd.y : tag, pc < 4 ? gd.z : __float_as_uint(L), pc < 4 ? gd.w : tag};
            __builtin_amdgcn_raw_buffer_store_b128(gv, rs, pc <= 4 ? GO_OFF + ((((ab * 8 + j) * 8 + as) * 4 + hl) * 64 + lane) * 16 : OOB, 0, SC1);
            if (wave == 7) TS(11);
        }
        TS(6);
        if (has2) {
            u32x2 bfr[8];
            transpose4x8(wf, bfr);
#pragma unroll
            for (int e = 0; e < 8; ++e) asm volatile("" : "+v"(bfr[e].x), "+v"(bfr[e].y));
            u32x2 af;
            const int lb = c >> 2, sq = c & 3;
            if (lowh) {
                const int head = rg >> 1, hl = head & 3;
                const int offl = GO_OFF + ((((lb * 8 + j) * 8 + sq) * 4 + hl) * 8 + 4 * (rg & 1) + w4) * 128;
                u32x4 g0 = {0u, 0u, 0u, 0u}, g1 = g0, m0 = g0, m1 = g0;
                bool ok = false;
                __builtin_amdgcn_s_sleep(H2_DELAY);
                for (unsigned spins = 0;;) {
                    CBAR();
                    if (!ok) {
                        g0 = __builtin_amdgcn_raw_buffer_load_b128(rs, offl + g * 16, 0, SC1);
                        g1 = __builtin_amdgcn_raw_buffer_load_b128(rs, offl + g * 16 + 16384, 0, SC1);
                        m0 = __builtin_amdgcn_raw_buffer_load_b128(rs, offl + 64, 0, SC1);
                        m1 = __builtin_amdgcn_raw_buffer_load_b128(rs, offl + 64 + 16384, 0, SC1);
                        ok = ((pk24_bad(g0, tg) | pk24_bad(g1, tg)) | (((m0.y ^ tag) | (m0.w ^ tag)) | ((m1.y ^ tag) | (m1.w ^ tag)))) == 0u;
                    }
                    if (__all(ok) || ++spins > SPIN_CAP) break;
                    __builtin_amdgcn_s_sleep(H2_SLEEP);
                }
                const float ma = __uint_as_float(m0.x), mb = __uint_as_float(m1.x);
                const float MM = quad_max(fmaxf(ma, mb));
                const float w0 = __builtin_amdgcn_exp2f((ma - MM) * LOG2E), w1_ = __builtin_amdgcn_exp2f((mb - MM) * LOG2E);
                const float LL = quad_sum(fmaf(w0, __uint_as_float(m0.z), w1_ * __uint_as_float(m1.z)));
                const float inv = 1.0f / LL;
                const f32x4 oa = up24(g0), ob = up24(g1);
                const float v0 = quad_sum(fmaf(w0, oa[0], w1_ * ob[0])) * inv, v1 = quad_sum(fmaf(w0, oa[1], w1_ * ob[1])) * inv;
                const float v2 = quad_sum(fmaf(w0, oa[2], w1_ * ob[2])) * inv, v3 = quad_sum(fmaf(w0, oa[3], w1_ * ob[3])) * inv;
                af = u32x2{pack_bf16(v0, v1), pack_bf16(v2, v3)};
            } else {
                const int offh = GH_OFF + ((rg2 * 4 + sq) * 4 + lb) * 256 + (8 * w4 + 2 * g) * 8;
                u32x4 gv;
                for (unsigned spins = 0;;) {
                    CBAR();
                    gv = __builtin_amdgcn_raw_buffer_load_b128(rs, offh, 0, SC1);
                    if (__all(pk24_bad(gv, tg) == 0u) || ++spins > SPIN_CAP) break;
                    __builtin_amdgcn_s_sleep(H2_SLEEP);
                }
                const f32x4 hp = up24(gv);
                float zz[4] = {quad_sum(hp[0]), quad_sum(hp[1]), quad_sum(hp[2]), quad_sum(hp[3])};
                const float bb[4] = {bf_lo(b1v.x), bf_hi(b1v.x), bf_lo(b1v.y), bf_hi(b1v.y)};
#pragma unroll
                for (int e = 0; e < 4; ++e) {
                    const float z = rbf(rbf(zz[e]) + bb[e]);
                    const float u = 0.7978845608028654f * (z + 0.044715f * z * z * z);
                    const float th = 1.0f - 2.0f / (__expf(2.0f * u) + 1.0f);
                    zz[e] = 0.5f * z * (1.0f + th);
                }
                af = u32x2{pack_bf16(zz[0], zz[1]), pack_bf16(zz[2], zz[3])};
            }
            TS(7);
            const f32x4 z = {0.f, 0.f, 0.f, 0.f};
            float o8[8];
#pragma unroll
            for (int e = 0; e < 8; ++e) o8[e] = mfma16(af, bfr[e], z)[0];
            float* dst = reinterpret_cast<float*>(smem + L_WP) + (wave * 4 + g) * 128 + c * 8;
            *reinterpret_cast<f32x4*>(dst) = f32x4{o8[0], o8[1], o8[2], o8[3]};
            *reinterpret_cast<f32x4*>(dst + 4) = f32x4{o8[4], o8[5], o8[6], o8[7]};
        }
        __syncthreads();
        if (has2 && w4 < 2) {
            const int t7 = w4 * 64 + lane, r = t7 >> 1, u2 = t7 & 1, wb = wave & 4;
            f32x2 s0 = {0.f, 0.f}, s1 = {0.f, 0.f};
#pragma unroll
            for (int w = 0; w < 4; ++w) {
                s0 += *reinterpret_cast<const f32x2*>(smem + L_WP + (((wb + w) * 4 + 2 * u2) * 128 + 2 * r) * 4);
                s1 += *reinterpret_cast<const f32x2*>(smem + L_WP + (((wb + w) * 4 + 2 * u2 + 1) * 128 + 2 * r) * 4);
            }
            const int cgx = lowh ? cgp : cg2, slot3 = lowh ? rg : 64 + rg2;
            __builtin_amdgcn_raw_buffer_store_b128(pk24(s0.x, s0.y, s1.x, s1.y, tg), rs, G3_OFF + ((cgx * 96 + slot3) * 128 + t7) * 16, 0, SC1);
        }
        TS(8);
        if (wave == FINW) {
            const int cgf = j >> 1, rr = (j & 1) * 32 + i;
            const int pp = lane >> 1, u2 = lane & 1;
            const int e = ((lane >> 5) << 1) | ((lane >> 4) & 1), ob = 2 * u2 + (e >> 1), oc = e & 1;
            const float gt = reinterpret_cast<const float*>(smem + L_ST)[ob * 4 + 3];
            const float b2f = oc ? bf_hi(b2v) : bf_lo(b2v);
            const float omg = rbf(1.0f - gt);
            const int off = G3_OFF + (((cgf * 96 + pp) * 64 + rr) * 2 + u2) * 16;
            u32x4 gv[3];
#pragma unroll
            for (int u = 0; u < 3; ++u) gv[u] = u32x4{0u, 0u, 0u, 0u};
            bool ok = false;
            __builtin_amdgcn_s_sleep(H3_DELAY);
            for (unsigned spins = 0;;) {
                CBAR();
                if (!ok) {
#pragma unroll
                    for (int u = 0; u < 3; ++u) gv[u] = __builtin_amdgcn_raw_buffer_load_b128(rs, off + u * (32 * 2048), 0, SC1);
                    u32 bad = pk24_bad(gv[0], tg) | pk24_bad(gv[1], tg);
                    if (pp < 28) bad |= pk24_bad(gv[2], tg);
                    ok = bad == 0u;
                }
                if (__all(ok) || ++spins > SPIN_CAP) break;
                __builtin_amdgcn_s_sleep(H3_SLEEP);
            }
            const f32x4 av = up24(gv[0]) + up24(gv[1]);
            f32x4 cv = up24(gv[2]);
            if (pp >= 28) cv = f32x4{0.f, 0.f, 0.f, 0.f};
            const bool isc = (lane & 2) != 0;
            float v[4];
#pragma unroll
            for (int k = 0; k < 4; ++k) { const float keep = isc ? cv[k] : av[k], give = isc ? av[k] : cv[k]; v[k] = keep + dppf<XOR2>(give); }
            auto sw0 = __builtin_amdgcn_permlane32_swap(__float_as_uint(v[0]), __float_as_uint(v[2]), false, false);
            auto sw1 = __builtin_amdgcn_permlane32_swap(__float_as_uint(v[1]), __float_as_uint(v[3]), false, false);
            const float x0 = __uint_as_float(sw0[0]) + __uint_as_float(sw0[1]), x1 = __uint_as_float(sw1[0]) + __uint_as_float(sw1[1]);
            auto sw2 = __builtin_amdgcn_permlane16_swap(__float_as_uint(x0), __float_as_uint(x1), false, false);
            float y = __uint_as_float(sw2[0]) + __uint_as_float(sw2[1]);
            y = ror_sum(y);
            const float other = dppf<XOR2>(y);
            const float res = rbf(gt * rbf(y)) + rbf(omg * rbf(rbf(other) + b2f));
            if ((lane & 14) == 0) out[ob * 512 + 64 * j + 2 * i + oc] = __builtin_bit_cast(u16, (__bf16)res);
            if (bid == 0 && lane == 0) __builtin_amdgcn_raw_buffer_store_b32(tag, rs, E_OFF, 0, SC1);
            TS(9);
        }
        TS_FLUSH();
    } else {
        const u32 ev = __builtin_amdgcn_raw_buffer_load_b32(rs, E_OFF, 0, SC1);
        if (wave > 8) {
            const int aw = wave - 9, ta = j * 14 + i, kq = ta & 3, cb = ta >> 2, r0 = 128 * kq + 32 * aw;
            const u32x4 xrow = *reinterpret_cast<const u32x4*>(x + aw * 512 + lane * 8);
            const u32x4 gwv = *reinterpret_cast<const u32x4*>(gw + lane * 8);
            const u32x2 xa = *reinterpret_cast<const u32x2*>(x + (c >> 2) * 512 + r0 + (g * 2 + ((c >> 1) & 1)) * 4);
            const __amdgpu_buffer_rsrc_t r1 = __builtin_amdgcn_make_buffer_rsrc((void*)w1, 0, 512 * 1792 * 2, 0x00020000);
            const int off1 = setA ? (r0 + (g * 2 + (c >> 3)) * 4) * 3584 + (64 * cb + (c & 7) * 8) * 2 : OOB;
            u32x4 t2[4];
#pragma unroll
            for (int t = 0; t < 4; ++t) t2[t] = __builtin_amdgcn_raw_buffer_load_b128(r1, off1 + t * 3584, 0, NT);
            SB();
            __syncthreads();
            SB();
            float f[8];
#pragma unroll
            for (int e = 0; e < 4; ++e) { f[2 * e] = bf_lo(xrow[e]); f[2 * e + 1] = bf_hi(xrow[e]); }
            const float mean = wave_sum(((f[0] + f[1]) + (f[2] + f[3])) + ((f[4] + f[5]) + (f[6] + f[7]))) * (1.0f / 512.0f);
            float vs = 0.f, gsum = 0.f;
#pragma unroll
            for (int e = 0; e < 4; ++e) {
                const float d0 = f[2 * e] - mean, d1 = f[2 * e + 1] - mean; vs = fmaf(d0, d0, vs); vs = fmaf(d1, d1, vs);
                gsum += rbf(f[2 * e] * bf_lo(gwv[e])); gsum += rbf(f[2 * e + 1] * bf_hi(gwv[e]));
            }
            const float var = wave_sum(vs) * (1.0f / 512.0f);
            const float gg = rbf(wave_sum(gsum));
            const float sd = rbf(sqrtf(rbf(rbf(var) + 1e-5f)));
            if (lane == 0) *reinterpret_cast<f32x4*>(smem + L_ST + aw * 16) = f32x4{rbf(mean), sd, 1.0f / sd, rbf(1.0f / (1.0f + __expf(-gg)))};
            LDS_FENCE();
            if (lane == 0) __hip_atomic_fetch_add(cnt + 2, 1u, __ATOMIC_RELAXED, __HIP_MEMORY_SCOPE_WORKGROUP);
            if (setA) {
                while (LDS_LD(cnt + 2) < 4u) __builtin_amdgcn_s_sleep(1);
                CBAR();
                const f32x4 stt = *reinterpret_cast<const f32x4*>(smem + L_ST + (c >> 2) * 16);
                float d[4] = {rbf(bf_lo(xa.x) - stt[0]), rbf(bf_hi(xa.x) - stt[0]), rbf(bf_lo(xa.y) - stt[0]), rbf(bf_hi(xa.y) - stt[0])};
#pragma unroll
                for (int e = 0; e < 4; ++e) { const float q0 = d[e] * stt[2]; d[e] = fmaf(fmaf(-q0, stt[1], d[e]), stt[2], q0); }
                const u32 tag = __builtin_amdgcn_readfirstlane(ev) + 1u;
                tile_phase(smem, rs, t2, u32x2{pack_bf16(d[0], d[1]), pack_bf16(d[2], d[3])}, 4 + aw, 1, GH_OFF + (cb * 4 + kq) * 1024, make_tag2(tag), lane);
            }
            return;
        }
        if (lane < 8) cnt[lane] = 0u;
        SB();
        __syncthreads();
        SB();
        TS(1);
        const u32 tag = __builtin_amdgcn_readfirstlane(ev) + 1u;
        const Tag2 tg = make_tag2(tag);
        while (LDS_LD(cnt + 0) < 4u) __builtin_amdgcn_s_sleep(2);
        while (LDS_LD(cnt + 5) < KTRIG) __builtin_amdgcn_s_sleep(1);
        while (__builtin_amdgcn_s_memrealtime() - tstart < QDELAY) __builtin_amdgcn_s_sleep(1);
        if (wave > 8) __builtin_amdgcn_s_sleep(QSLEEP * 2);
        if (wave > 9) __builtin_amdgcn_s_sleep(QSLEEP * 2);
        const int off = GQ_OFF + (((j * 8 + (lane >> 3)) * 4) * 4 + ab) * 256 + (lane & 7) * 32;
        for (unsigned spins = 0;; ++spins) {
            CBAR();
            u32x4 pa[4], pb[4];
#pragma unroll
            for (int k = 0; k < 4; ++k) { pa[k] = __builtin_amdgcn_raw_buffer_load_b128(rs, off + k * 1024, 0, SC1); pb[k] = __builtin_amdgcn_raw_buffer_load_b128(rs, off + k * 1024 + 16, 0, SC1); }
            SB();
            if (spins < 2 && lane == 0) __hip_atomic_store(cnt + 6, spins + 1u, __ATOMIC_RELAXED, __HIP_MEMORY_SCOPE_WORKGROUP);
            SB();
            u32 bad = 0;
#pragma unroll
            for (int k = 0; k < 4; ++k) bad |= pk24_bad(pa[k], tg) | pk24_bad(pb[k], tg);
            if (__all(bad == 0u) || spins > SPIN_CAP) {
                const f32x4 qa = (up24(pa[0]) + up24(pa[1])) + (up24(pa[2]) + up24(pa[3])), qb = (up24(pb[0]) + up24(pb[1])) + (up24(pb[2]) + up24(pb[3]));
                const u32x4 qv = {pack_bf16(qa[0], qa[1]), pack_bf16(qa[2], qa[3]), pack_bf16(qb[0], qb[1]), pack_bf16(qb[2], qb[3])};
                *reinterpret_cast<u32x4*>(smem + L_QS + lane * 16) = qv;
                LDS_FENCE();
                if (lane == 0) { __hip_atomic_store(cnt + 3, 1u, __ATOMIC_RELAXED, __HIP_MEMORY_SCOPE_WORKGROUP); __hip_atomic_store(cnt + 6, 2u, __ATOMIC_RELAXED, __HIP_MEMORY_SCOPE_WORKGROUP); }
                break;
            }
        }
        TS(3);
        TS_FLUSH();
    }
}

extern "C" void kernel_launch(void* const* d_in, const int* in_sizes, int n_in,
                              void* d_out, int out_size, void* d_ws, size_t ws_size,
                              hipStream_t stream) {
    fused_decode<<<256, NTHREADS, 0, stream>>>((const u16*)d_in[0], (const u16*)d_in[1], (const u16*)d_in[2],
                                               (const u16*)d_in[3], (const u16*)d_in[4], (const u16*)d_in[6],
                                               (const u16*)d_in[7], (const u16*)d_in[8], (const u16*)d_in[9],
                                               (const u16*)d_in[10], (u16*)d_out, (unsigned char*)d_ws);
}
```

```cpp
#include <hip/hip_runtime.h>
#include <hip/hip_bf16.h>
#include <stdint.h>

typedef unsigned short u16;
typedef unsigned int u32;
typedef u32 u32x2 __attribute__((ext_vector_type(2)));
typedef u32 u32x4 __attribute__((ext_vector_type(4)));
typedef float f32x2 __attribute__((ext_vector_type(2)));
typedef float f32x4 __attribute__((ext_vector_type(4)));
typedef short s16x4 __attribute__((ext_vector_type(4)));
typedef __bf16 bf16x8 __attribute__((ext_vector_type(8)));
typedef __bf16 bf16x2 __attribute__((ext_vector_type(2)));

#define NPOLL 1
#define WEARLY 4
#define QPRIO 0
#define VHOLD 1
#define QDELAY 260
#define VDELAY 0
#define QSLEEP 4
#define SCHED 1
#define H2_DELAY 24
#define H3_DELAY 20
#define H2_SLEEP 2
#define H3_SLEEP 1
#define H2_DIRECT 16
constexpr int NTHREADS = (8 + NPOLL) * 64;

constexpr int E_OFF = 0;
constexpr int GQ_OFF = 4096;
constexpr int GH_OFF = GQ_OFF + 262144;
constexpr int GO_OFF = GH_OFF + 131072;
constexpr int G3_OFF = GO_OFF + 1048576;
constexpr int WS_USED = G3_OFF + 4 * 96 * 64 * 64;
constexpr int OOB = 0x40000000;

constexpr int L_TP = 0;
constexpr int L_ST = L_TP + 8192;
constexpr int L_CNT = L_ST + 64;
constexpr int L_QS = L_CNT + 64;
constexpr int L_MX = L_QS + 1024;
constexpr int L_LS = L_MX + 128;
constexpr int L_OP = L_LS + 128;
constexpr int L_WP = L_OP + 16384;
constexpr int L_END = L_WP + 16384;
constexpr int LDS_BYTES = 84 * 1024;
static_assert(L_END <= LDS_BYTES, "lds");

constexpr unsigned SPIN_CAP = 1u << 18;
constexpr float LOG2E = 1.4426950408889634f;

__device__ __forceinline__ float bf_lo(u32 v) { return __uint_as_float(v << 16); }
__device__ __forceinline__ float bf_hi(u32 v) { return __uint_as_float(v & 0xffff0000u); }
__device__ __forceinline__ u32 pack_bf16(float lo, float hi) {
    bf16x2 v = {(__bf16)lo, (__bf16)hi};
    return __builtin_bit_cast(u32, v);
}
__device__ __forceinline__ float rbf(float f) { return bf_lo((u32)__builtin_bit_cast(u16, (__bf16)f)); }

template <int CTRL> __device__ __forceinline__ float dppf(float x) {
    return __builtin_bit_cast(float, __builtin_amdgcn_mov_dpp(__builtin_bit_cast(int, x), CTRL, 0xf, 0xf, true));
}
constexpr int XOR1 = 0xB1, XOR2 = 0x4E, ROR4 = 0x124, ROR8 = 0x128;
__device__ __forceinline__ float quad_sum(float x) { x += dppf<XOR1>(x); x += dppf<XOR2>(x); return x; }
__device__ __forceinline__ float quad_max(float x) { x = fmaxf(x, dppf<XOR1>(x)); x = fmaxf(x, dppf<XOR2>(x)); return x; }
__device__ __forceinline__ float ror_sum(float x) { x += dppf<ROR8>(x); x += dppf<ROR4>(x); return x; }
__device__ __forceinline__ float xrow_max(float x) {
    auto s = __builtin_amdgcn_permlane16_swap(__float_as_uint(x), __float_as_uint(x), false, false);
    x = fmaxf(__uint_as_float(s[0]), __uint_as_float(s[1]));
    auto t = __builtin_amdgcn_permlane32_swap(__float_as_uint(x), __float_as_uint(x), false, false);
    return fmaxf(__uint_as_float(t[0]), __uint_as_float(t[1]));
}
__device__ __forceinline__ float xrow_sum(float x) {
    auto s = __builtin_amdgcn_permlane16_swap(__float_as_uint(x), __float_as_uint(x), false, false);
    x = __uint_as_float(s[0]) + __uint_as_float(s[1]);
    auto t = __builtin_amdgcn_permlane32_swap(__float_as_uint(x), __float_as_uint(x), false, false);
    return __uint_as_float(t[0]) + __uint_as_float(t[1]);
}
__device__ __forceinline__ float wave_sum(float x) { return xrow_sum(ror_sum(quad_sum(x))); }

__device__ __forceinline__ u32 plo(u32 a, u32 b) { return __builtin_amdgcn_perm(b, a, 0x05040100u); }
__device__ __forceinline__ u32 phi(u32 a, u32 b) { return __builtin_amdgcn_perm(b, a, 0x07060302u); }

__device__ __forceinline__ f32x4 mfma16(u32x2 a, u32x2 b, f32x4 c) {
    return __builtin_amdgcn_mfma_f32_16x16x16bf16_1k(__builtin_bit_cast(s16x4, a), __builtin_bit_cast(s16x4, b), c, 0, 0, 0);
}
__device__ __forceinline__ f32x4 mfma32(u32x4 a, u32x4 b, f32x4 c) {
    return __builtin_amdgcn_mfma_f32_16x16x32_bf16(__builtin_bit_cast(bf16x8, a), __builtin_bit_cast(bf16x8, b), c, 0, 0, 0);
}
__device__ __forceinline__ float sel4(f32x4 d, int s) {
    float lo = (s & 1) ? d[1] : d[0], hi = (s & 1) ? d[3] : d[2];
    return (s & 2) ? hi : lo;
}
__device__ __forceinline__ void transpose4x8(const u32x4 (&w)[4], u32x2 (&f)[8]) {
#pragma unroll
    for (int r = 0; r < 4; ++r) {
        f[2 * r] = u32x2{plo(w[0][r], w[1][r]), plo(w[2][r], w[3][r])};
        f[2 * r + 1] = u32x2{phi(w[0][r], w[1][r]), phi(w[2][r], w[3][r])};
    }
}
struct Tag2 { u32 t1, t2; };
__device__ __forceinline__ Tag2 make_tag2(u32 tag) { return Tag2{tag & 0xffffu, ((tag * 40503u) ^ (tag >> 16)) & 0xffffu}; }
__device__ __forceinline__ u32x4 pk24(float a, float b, float c, float d, Tag2 t) {
    const u32 ua = __float_as_uint(a) + 0x80u, ub = __float_as_uint(b) + 0x80u, uc = __float_as_uint(c) + 0x80u, ud = __float_as_uint(d) + 0x80u;
    u32x4 r;
    r.x = __builtin_amdgcn_perm(ua, ub, 0x07060503u);
    r.y = (__builtin_amdgcn_perm(ub, ub, 0x02010c0cu)) | t.t1;
    r.z = __builtin_amdgcn_perm(uc, ud, 0x07060503u);
    r.w = (__builtin_amdgcn_perm(ud, ud, 0x02010c0cu)) | t.t2;
    return r;
}
__device__ __forceinline__ u32 pk24_bad(u32x4 u, Tag2 t) { return ((u.y ^ t.t1) | (u.w ^ t.t2)) & 0xffffu; }
__device__ __forceinline__ f32x4 up24(u32x4 u) {
    return f32x4{__uint_as_float(u.x & 0xffffff00u), __uint_as_float(__builtin_amdgcn_perm(u.x, u.y, 0x0403020cu)),
                 __uint_as_float(u.z & 0xffffff00u), __uint_as_float(__builtin_amdgcn_perm(u.z, u.w, 0x0403020cu))};
}
#define SC1 16
#define NT 2
#define CBAR() asm volatile("" ::: "memory")
#define KEEP4(v) asm volatile("" : "+v"((v).x), "+v"((v).y), "+v"((v).z), "+v"((v).w))
#define LDS_FENCE() asm volatile("s_waitcnt lgkmcnt(0)" ::: "memory")
#define LDS_LD(p) __hip_atomic_load((p), __ATOMIC_RELAXED, __HIP_MEMORY_SCOPE_WORKGROUP)
#define SB() __builtin_amdgcn_sched_barrier(0)

#define TS(k)
#define TS_DECL
#define TS_FLUSH()

__global__ void __launch_bounds__(NTHREADS, 3)
fused_decode(const u16* __restrict__ x, const u16* __restrict__ kc, const u16* __restrict__ vc,
             const u16* __restrict__ wq, const u16* __restrict__ wo, const u16* __restrict__ w1,
             const u16* __restrict__ b1, const u16* __restrict__ w2, const u16* __restrict__ b2,
             const u16* __restrict__ gw, u16* __restrict__ out, unsigned char* ws) {
    __shared__ __attribute__((aligned(16))) unsigned char smem[LDS_BYTES];
    const int tid = threadIdx.x, lane = tid & 63;
    const int wave = __builtin_amdgcn_readfirstlane(tid >> 6);
    const int bid = blockIdx.x, j = bid & 7, i = bid >> 3;
    const bool setA = i < 14, setB = (i >= 14) && (i < 28);
    const int g = lane >> 4, c = lane & 15;
    const __amdgpu_buffer_rsrc_t rs = __builtin_amdgcn_make_buffer_rsrc(ws, 0, WS_USED, 0x00020000);
    const int ab = i >> 3, as = i & 7;
    const int rg = 8 * j + (i >> 2), cgp = i & 3;
    const int tb = j * 14 + (i - 14), rg2 = tb >> 2, cg2 = tb & 3;
    u32* const cnt = reinterpret_cast<u32*>(smem + L_CNT);
    TS_DECL
    TS(0);
    const unsigned long long tstart = __builtin_amdgcn_s_memrealtime();

    if (wave < 8) {
        const bool lowh = wave < 4;
        const int w4 = wave & 3;
        const int ta = j * 14 + i;
        const int kq = lowh ? (i >> 3) : (ta & 3), cb = lowh ? (i & 7) : (ta >> 2);
        const int r0 = 128 * kq + 32 * w4;
        const __amdgpu_buffer_rsrc_t r1 = __builtin_amdgcn_make_buffer_rsrc((void*)(lowh ? wq : w1), 0, lowh ? 512 * 4096 * 2 : 512 * 1792 * 2, 0x00020000);
        const int ldw = lowh ? 8192 : 3584;
        const int off1 = (lowh || setA) ? (r0 + (g * 2 + (c >> 3)) * 4) * ldw + ((lowh ? 512 * j : 0) + 64 * cb + (c & 7) * 8) * 2 : OOB;
        u32x4 t1[4], t2[4];
#pragma unroll
        for (int t = 0; t < 4; ++t) t1[t] = __builtin_amdgcn_raw_buffer_load_b128(r1, (lowh ? off1 : OOB) + t * 8192, 0, NT);
        const u32x2 xa = *reinterpret_cast<const u32x2*>(x + (c >> 2) * 512 + r0 + (g * 2 + ((c >> 1) & 1)) * 4);
        const bool has2 = lowh || setB;
        const __amdgpu_buffer_rsrc_t r2 = __builtin_amdgcn_make_buffer_rsrc((void*)(lowh ? wo : w2), 0, lowh ? 4096 * 512 * 2 : 1792 * 512 * 2, 0x00020000);
        const int off2 = has2 ? ((64 * (lowh ? rg : rg2) + 16 * w4 + 4 * g) * 512 + 128 * (lowh ? cgp : cg2) + 8 * c) * 2 : OOB;
        u32x4 kf[4], vf[4], wf[4];
        const u32 ev = __builtin_amdgcn_raw_buffer_load_b32(rs, E_OFF, 0, SC1);
        const __amdgpu_buffer_rsrc_t rb1 = __builtin_amdgcn_make_buffer_rsrc((void*)b1, 0, 1792 * 2, 0x00020000);
        const u32x2 b1v = __builtin_amdgcn_raw_buffer_load_b64(rb1, (!lowh && setB) ? (64 * rg2 + 16 * w4 + 4 * g) * 2 : OOB, 0, 0);
        const u32 b2v = *reinterpret_cast<const u32*>(b2 + 64 * j + 2 * i);
        const __amdgpu_buffer_rsrc_t rk = __builtin_amdgcn_make_buffer_rsrc((void*)kc, 0, 32 << 20, 0x00020000);
        const __amdgpu_buffer_rsrc_t rv = __builtin_amdgcn_make_buffer_rsrc((void*)vc, 0, 32 << 20, 0x00020000);
        const int kvb = ((ab * 8 + j) * 4096 + 3072 + 128 * as + 16 * wave) * 256;
        const int offk = kvb + (4 * ((lane >> 2) & 3)) * 256 + (g * 4 + (lane & 3)) * 16;
        const int offv = kvb + (4 * g) * 256 + c * 16;
        SB();
#pragma unroll
        for (int t = 0; t < 4; ++t) t2[t] = __builtin_amdgcn_raw_buffer_load_b128(r1, (lowh ? OOB : off1) + t * 3584, 0, NT);
#pragma unroll
        for (int t = 0; t < WEARLY; ++t) wf[t] = __builtin_amdgcn_raw_buffer_load_b128(r2, off2 + t * 1024, 0, NT);
        SB();
        __syncthreads();
        SB();
        const u32 tag = __builtin_amdgcn_readfirstlane(ev) + 1u;
        const Tag2 tg = make_tag2(tag);

        auto tile_phase = [&](const u32x4 (&tw)[4], u32x2 af, int cidx, int goff) {
            u32x2 f[8];
            transpose4x8(tw, f);
            const f32x4 z = {0.f, 0.f, 0.f, 0.f};
            float v[8];
#pragma unroll
            for (int e = 0; e < 8; ++e) { const f32x4 d = mfma16(af, f[e], z); const float s = (c & 8) ? d[2] : d[0]; v[e] = s + dppf<ROR8>(s); }
            if (c < 8) {
                float* dst = reinterpret_cast<float*>(smem + L_TP) + (wave * 4 + g) * 64 + c * 8;
                *reinterpret_cast<f32x4*>(dst) = f32x4{v[0], v[1], v[2], v[3]};
                *reinterpret_cast<f32x4*>(dst + 4) = f32x4{v[4], v[5], v[6], v[7]};
            }
            LDS_FENCE();
            u32 old = 0;
            if (lane == 0) old = __hip_atomic_fetch_add(cnt + cidx, 1u, __ATOMIC_RELAXED, __HIP_MEMORY_SCOPE_WORKGROUP);
            old = __builtin_amdgcn_readfirstlane(old);
            if (old == 3u) {
                CBAR();
                const float* src = reinterpret_cast<const float*>(smem + L_TP) + (wave & 4) * 256 + lane * 4;
                f32x4 a = *reinterpret_cast<const f32x4*>(src);
#pragma unroll
                for (int w = 1; w < 4; ++w) a += *reinterpret_cast<const f32x4*>(src + w * 256);
                __builtin_amdgcn_raw_buffer_store_b128(pk24(a[0], a[1], a[2], a[3], tg), rs, goff + lane * 16, 0, SC1);
                if (cidx == 0) TS(10);
            }
        };
        if (lowh) tile_phase(t1, xa, 0, GQ_OFF + ((j * 8 + cb) * 4 + kq) * 1024);
        TS(1);
        while (LDS_LD(cnt + 0) < 4u) __builtin_amdgcn_s_sleep(1);
        CBAR();
#pragma unroll
        for (int t = 0; t < 4; ++t) kf[t] = __builtin_amdgcn_raw_buffer_load_b128(rk, offk + t * 256, 0, NT);
        SB();
        KEEP4(kf[0]); KEEP4(kf[1]);
        while (LDS_LD(cnt + 6) == 0u) __builtin_amdgcn_s_sleep(1);
        CBAR();
        vf[0] = __builtin_amdgcn_raw_buffer_load_b128(rv, offv, 0, NT); vf[1] = __builtin_amdgcn_raw_buffer_load_b128(rv, offv + 256, 0, NT);
        SB();
        KEEP4(kf[2]); KEEP4(kf[3]);
        vf[2] = __builtin_amdgcn_raw_buffer_load_b128(rv, offv + 512, 0, NT); vf[3] = __builtin_amdgcn_raw_buffer_load_b128(rv, offv + 768, 0, NT);
        SB();
        TS(2);
        if (!lowh && setA) {
            while (LDS_LD(cnt + 2) == 0u) __builtin_amdgcn_s_sleep(1);
            CBAR();
            const f32x4 stt = *reinterpret_cast<const f32x4*>(smem + L_ST + (c >> 2) * 16);
            float d[4] = {rbf(bf_lo(xa.x) - stt[0]), rbf(bf_hi(xa.x) - stt[0]), rbf(bf_lo(xa.y) - stt[0]), rbf(bf_hi(xa.y) - stt[0])};
#pragma unroll
            for (int e = 0; e < 4; ++e) { const float q0 = d[e] * stt[2]; d[e] = fmaf(fmaf(-q0, stt[1], d[e]), stt[2], q0); }
            tile_phase(t2, u32x2{pack_bf16(d[0], d[1]), pack_bf16(d[2], d[3])}, 1, GH_OFF + (cb * 4 + kq) * 1024);
        }
        TS(3);
        while (LDS_LD(cnt + 3) == 0u) __builtin_amdgcn_s_sleep(1);
        CBAR();
        TS(4);
#pragma unroll
        for (int t = WEARLY; t < 4; ++t) wf[t] = __builtin_amdgcn_raw_buffer_load_b128(r2, off2 + t * 1024, 0, NT);
        SB();
        float sc[4];
        {
            const u32x4 qf = *reinterpret_cast<const u32x4*>(smem + L_QS + (((lane >> 2) & 3) * 128 + (g * 4 + (lane & 3)) * 8) * 2);
            const f32x4 z = {0.f, 0.f, 0.f, 0.f};
#pragma unroll
            for (int t = 0; t < 4; ++t) {
                const f32x4 d = mfma32(kf[t], qf, z);
                sc[t] = rbf(rbf(quad_sum(sel4(d, lane & 3))) * 0.08838834764831845f);
            }
            const float m = xrow_max(fmaxf(fmaxf(sc[0], sc[1]), fmaxf(sc[2], sc[3])));
            if (g == 0 && (c & 3) == 0) reinterpret_cast<float*>(smem + L_MX)[(c >> 2) * 8 + wave] = m;
        }
        LDS_FENCE();
        if (lane == 0) __hip_atomic_fetch_add(cnt + 4, 1u, __ATOMIC_RELAXED, __HIP_MEMORY_SCOPE_WORKGROUP);
        while (LDS_LD(cnt + 4) < 8u) {}
        CBAR();
        float M;
        u32x2 pa;
        {
            const f32x4 ma = *reinterpret_cast<const f32x4*>(smem + L_MX + (c >> 2) * 32), mb = *reinterpret_cast<const f32x4*>(smem + L_MX + (c >> 2) * 32 + 16);
            M = fmaxf(fmaxf(fmaxf(ma[0], ma[1]), fmaxf(ma[2], ma[3])), fmaxf(fmaxf(mb[0], mb[1]), fmaxf(mb[2], mb[3])));
            const float p0 = __builtin_amdgcn_exp2f((sc[0] - M) * LOG2E), p1 = __builtin_amdgcn_exp2f((sc[1] - M) * LOG2E);
            const float p2 = __builtin_amdgcn_exp2f((sc[2] - M) * LOG2E), p3 = __builtin_amdgcn_exp2f((sc[3] - M) * LOG2E);
            pa = u32x2{pack_bf16(p0, p1), pack_bf16(p2, p3)};
            const float l = xrow_sum((p0 + p1) + (p2 + p3));
            if (g == 0 && (c & 3) == 0) reinterpret_cast<float*>(smem + L_LS)[(c >> 2) * 8 + wave] = l;
        }
        TS(5);
        {
            u32x2 f[8];
            transpose4x8(vf, f);
            const f32x4 z = {0.f, 0.f, 0.f, 0.f};
            float o8[8];
#pragma unroll
            for (int e = 0; e < 8; ++e) o8[e] = mfma16(pa, f[e], z)[0];
            float* dst = reinterpret_cast<float*>(smem + L_OP) + (wave * 4 + g) * 128 + c * 8;
            *reinterpret_cast<f32x4*>(dst) = f32x4{o8[0], o8[1], o8[2], o8[3]};
            *reinterpret_cast<f32x4*>(dst + 4) = f32x4{o8[4], o8[5], o8[6], o8[7]};
        }
        __syncthreads();
        if (wave >= 4) {
            const int hl = wave - 4, pc = lane & 7;
            const float* src = reinterpret_cast<const float*>(smem + L_OP) + hl * 128 + (lane >> 3) * 16 + (pc & 3) * 4;
            f32x4 a = *reinterpret_cast<const f32x4*>(src);
#pragma unroll
            for (int w = 1; w < 8; ++w) a += *reinterpret_cast<const f32x4*>(src + w * 512);
            const f32x4 la = *reinterpret_cast<const f32x4*>(smem + L_LS + hl * 32), lb_ = *reinterpret_cast<const f32x4*>(smem + L_LS + hl * 32 + 16);
            const f32x4 ma = *reinterpret_cast<const f32x4*>(smem + L_MX + hl * 32), mb = *reinterpret_cast<const f32x4*>(smem + L_MX + hl * 32 + 16);
            const float L = ((la[0] + la[1]) + (la[2] + la[3])) + ((lb_[0] + lb_[1]) + (lb_[2] + lb_[3]));
            const float Mh = fmaxf(fmaxf(fmaxf(ma[0], ma[1]), fmaxf(ma[2], ma[3])), fmaxf(fmaxf(mb[0], mb[1]), fmaxf(mb[2], mb[3])));
            const u32x4 gd = pk24(a[0], a[1], a[2], a[3], tg);
            const u32x4 gv = {pc < 4 ? gd.x : __float_as_uint(Mh), pc < 4 ? gd.y : tag, pc < 4 ? gd.z : __float_as_uint(L), pc < 4 ? gd.w : tag};
            __builtin_amdgcn_raw_buffer_store_b128(gv, rs, pc <= 4 ? GO_OFF + ((((ab * 8 + j) * 8 + as) * 4 + hl) * 64 + lane) * 16 : OOB, 0, SC1);
            if (wave == 7) TS(11);
        }
        TS(6);
        if (has2) {
            u32x2 bfr[8];
            transpose4x8(wf, bfr);
#pragma unroll
            for (int e = 0; e < 8; ++e) asm volatile("" : "+v"(bfr[e].x), "+v"(bfr[e].y));
            u32x2 af;
            const int lb = c >> 2, sq = c & 3;
            if (lowh) {
                const int head = rg >> 1, hl = head & 3;
                const int offl = GO_OFF + ((((lb * 8 + j) * 8 + sq) * 4 + hl) * 8 + 4 * (rg & 1) + w4) * 128;
                u32x4 g0 = {0u, 0u, 0u, 0u}, g1 = g0, m0 = g0, m1 = g0;
                bool ok = false;
                __builtin_amdgcn_s_sleep(H2_DELAY);
                for (unsigned spins = 0;;) {
                    CBAR();
                    if (!ok) {
                        g0 = __builtin_amdgcn_raw_buffer_load_b128(rs, offl + g * 16, 0, SC1);
                        g1 = __builtin_amdgcn_raw_buffer_load_b128(rs, offl + g * 16 + 16384, 0, SC1);
                        m0 = __builtin_amdgcn_raw_buffer_load_b128(rs, offl + 64, 0, SC1);
                        m1 = __builtin_amdgcn_raw_buffer_load_b128(rs, offl + 64 + 16384, 0, SC1);
                        ok = ((pk24_bad(g0, tg) | pk24_bad(g1, tg)) | (((m0.y ^ tag) | (m0.w ^ tag)) | ((m1.y ^ tag) | (m1.w ^ tag)))) == 0u;
                    }
                    if (__all(ok) || ++spins > SPIN_CAP) break;
                    __builtin_amdgcn_s_sleep(H2_SLEEP);
                }
                const float ma = __uint_as_float(m0.x), mb = __uint_as_float(m1.x);
                const float MM = quad_max(fmaxf(ma, mb));
                const float w0 = __builtin_amdgcn_exp2f((ma - MM) * LOG2E), w1_ = __builtin_amdgcn_exp2f((mb - MM) * LOG2E);
                const float LL = quad_sum(fmaf(w0, __uint_as_float(m0.z), w1_ * __uint_as_float(m1.z)));
                const float inv = 1.0f / LL;
                const f32x4 oa = up24(g0), ob = up24(g1);
                const float v0 = quad_sum(fmaf(w0, oa[0], w1_ * ob[0])) * inv, v1 = quad_sum(fmaf(w0, oa[1], w1_ * ob[1])) * inv;
                const float v2 = quad_sum(fmaf(w0, oa[2], w1_ * ob[2])) * inv, v3 = quad_sum(fmaf(w0, oa[3], w1_ * ob[3])) * inv;
                af = u32x2{pack_bf16(v0, v1), pack_bf16(v2, v3)};
            } else {
                const int offh = GH_OFF + ((rg2 * 4 + sq) * 4 + lb) * 256 + (8 * w4 + 2 * g) * 8;
                u32x4 gv;
                for (unsigned spins = 0;;) {
                    CBAR();
                    gv = __builtin_amdgcn_raw_buffer_load_b128(rs, offh, 0, SC1);
                    if (__all(pk24_bad(gv, tg) == 0u) || ++spins > SPIN_CAP) break;
                    __builtin_amdgcn_s_sleep(H2_SLEEP);
                }
                const f32x4 hp = up24(gv);
                float zz[4] = {quad_sum(hp[0]), quad_sum(hp[1]), quad_sum(hp[2]), quad_sum(hp[3])};
                const float bb[4] = {bf_lo(b1v.x), bf_hi(b1v.x), bf_lo(b1v.y), bf_hi(b1v.y)};
#pragma unroll
                for (int e = 0; e < 4; ++e) {
                    const float z = rbf(rbf(zz[e]) + bb[e]);
                    const float u = 0.7978845608028654f * (z + 0.044715f * z * z * z);
                    const float th = 1.0f - 2.0f / (__expf(2.0f * u) + 1.0f);
                    zz[e] = 0.5f * z * (1.0f + th);
                }
                af = u32x2{pack_bf16(zz[0], zz[1]), pack_bf16(zz[2], zz[3])};
            }
            TS(7);
            const f32x4 z = {0.f, 0.f, 0.f, 0.f};
            float o8[8];
#pragma unroll
            for (int e = 0; e < 8; ++e) o8[e] = mfma16(af, bfr[e], z)[0];
            float* dst = reinterpret_cast<float*>(smem + L_WP) + (wave * 4 + g) * 128 + c * 8;
            *reinterpret_cast<f32x4*>(dst) = f32x4{o8[0], o8[1], o8[2], o8[3]};
            *reinterpret_cast<f32x4*>(dst + 4) = f32x4{o8[4], o8[5], o8[6], o8[7]};
        }
        __syncthreads();
        if (has2) {
            const int t8 = tid & 255, r = t8 >> 2, b = t8 & 3, wb = wave & 4;
            float a0 = 0.f, a1 = 0.f;
#pragma unroll
            for (int w = 0; w < 4; ++w) {
                const f32x2 t = *reinterpret_cast<const f32x2*>(smem + L_WP + (((wb + w) * 4 + b) * 128 + 2 * r) * 4);
                a0 += t.x; a1 += t.y;
            }
            const int cgx = lowh ? cgp : cg2, slot3 = lowh ? rg : 64 + rg2;
            const u32x4 gv = {__float_as_uint(a0), tag, __float_as_uint(a1), tag};
            __builtin_amdgcn_raw_buffer_store_b128(gv, rs, G3_OFF + ((cgx * 96 + slot3) * 64 * 8 + t8 * 2) * 8, 0, SC1);
        }
        TS(8);
        if (wave == 0) {
            const int cgf = j >> 1, rr = (j & 1) * 32 + i;
            const int pp = lane >> 2, b = lane & 3;
            const float gt = reinterpret_cast<const float*>(smem + L_ST)[b * 4 + 3];
            const int off = G3_OFF + (((cgf * 96 + pp) * 64 + rr) * 8 + 2 * b) * 8;
            u32x4 gv[6];
#pragma unroll
            for (int u = 0; u < 6; ++u) gv[u] = u32x4{0u, 0u, 0u, 0u};
            bool ok = false;
            __builtin_amdgcn_s_sleep(H3_DELAY);
            for (unsigned spins = 0;;) {
                CBAR();
                if (!ok) {
#pragma unroll
                    for (int u = 0; u < 6; ++u) gv[u] = __builtin_amdgcn_raw_buffer_load_b128(rs, off + u * (16 * 4096), 0, SC1);
                    u32 bad = 0;
#pragma unroll
                    for (int u = 0; u < 5; ++u) bad |= (gv[u].y ^ tag) | (gv[u].w ^ tag);
                    if (pp < 12) bad |= (gv[5].y ^ tag) | (gv[5].w ^ tag);
                    ok = bad == 0u;
                }
                if (__all(ok) || ++spins > SPIN_CAP) break;
                __builtin_amdgcn_s_sleep(H3_SLEEP);
            }
            float a0 = (__uint_as_float(gv[0].x) + __uint_as_float(gv[1].x)) + (__uint_as_float(gv[2].x) + __uint_as_float(gv[3].x));
            float a1 = (__uint_as_float(gv[0].z) + __uint_as_float(gv[1].z)) + (__uint_as_float(gv[2].z) + __uint_as_float(gv[3].z));
            float c0 = __uint_as_float(gv[4].x) + ((pp < 12) ? __uint_as_float(gv[5].x) : 0.f);
            float c1 = __uint_as_float(gv[4].z) + ((pp < 12) ? __uint_as_float(gv[5].z) : 0.f);
            a0 = xrow_sum(ror_sum(a0)); a1 = xrow_sum(ror_sum(a1)); c0 = xrow_sum(ror_sum(c0)); c1 = xrow_sum(ror_sum(c1));
            const float omg = rbf(1.0f - gt);
            const float mb0 = rbf(rbf(c0) + bf_lo(b2v)), mb1 = rbf(rbf(c1) + bf_hi(b2v));
            const float r0_ = rbf(gt * rbf(a0)) + rbf(omg * mb0), r1_ = rbf(gt * rbf(a1)) + rbf(omg * mb1);
            if (lane < 4) *reinterpret_cast<u32*>(out + b * 512 + 64 * j + 2 * i) = pack_bf16(r0_, r1_);
            if (bid == 0 && lane == 0) __builtin_amdgcn_raw_buffer_store_b32(tag, rs, E_OFF, 0, SC1);
            TS(9);
        }
        TS_FLUSH();
    } else {
        const u32 ev = __builtin_amdgcn_raw_buffer_load_b32(rs, E_OFF, 0, SC1);
        if (wave == 8) {
            u32x4 xv[4];
#pragma unroll
            for (int b = 0; b < 4; ++b) xv[b] = *reinterpret_cast<const u32x4*>(x + b * 512 + lane * 8);
            const u32x4 gwv = *reinterpret_cast<const u32x4*>(gw + lane * 8);
            if (lane < 8) cnt[lane] = 0u;
            SB();
            __syncthreads();
            SB();
            float mean[4], var[4], gs[4];
#pragma unroll
            for (int b = 0; b < 4; ++b) {
                const float s = ((bf_lo(xv[b][0]) + bf_hi(xv[b][0])) + (bf_lo(xv[b][1]) + bf_hi(xv[b][1]))) + ((bf_lo(xv[b][2]) + bf_hi(xv[b][2])) + (bf_lo(xv[b][3]) + bf_hi(xv[b][3])));
                mean[b] = wave_sum(s) * (1.0f / 512.0f);
            }
#pragma unroll
            for (int b = 0; b < 4; ++b) {
                float vs = 0.f, s = 0.f;
#pragma unroll
                for (int e = 0; e < 4; ++e) {
                    const float d0 = bf_lo(xv[b][e]) - mean[b], d1 = bf_hi(xv[b][e]) - mean[b]; vs = fmaf(d0, d0, vs); vs = fmaf(d1, d1, vs);
                    s += rbf(bf_lo(xv[b][e]) * bf_lo(gwv[e])); s += rbf(bf_hi(xv[b][e]) * bf_hi(gwv[e]));
                }
                var[b] = wave_sum(vs) * (1.0f / 512.0f);
                gs[b] = wave_sum(s);
            }
            const float mn = (lane & 2) ? ((lane & 1) ? mean[3] : mean[2]) : ((lane & 1) ? mean[1] : mean[0]);
            const float vr = (lane & 2) ? ((lane & 1) ? var[3] : var[2]) : ((lane & 1) ? var[1] : var[0]);
            const float gg = rbf((lane & 2) ? ((lane & 1) ? gs[3] : gs[2]) : ((lane & 1) ? gs[1] : gs[0]));
            const float sd = rbf(sqrtf(rbf(rbf(vr) + 1e-5f)));
            if (lane < 4) *reinterpret_cast<f32x4*>(smem + L_ST + lane * 16) = f32x4{rbf(mn), sd, 1.0f / sd, rbf(1.0f / (1.0f + __expf(-gg)))};
            LDS_FENCE();
            if (lane == 0) __hip_atomic_store(cnt + 2, 1u, __ATOMIC_RELAXED, __HIP_MEMORY_SCOPE_WORKGROUP);
        } else {
            SB();
            __syncthreads();
            SB();
        }
        TS(1);
        const u32 tag = __builtin_amdgcn_readfirstlane(ev) + 1u;
        const Tag2 tg = make_tag2(tag);
        while (LDS_LD(cnt + 0) < 4u) __builtin_amdgcn_s_sleep(2);
        while (__builtin_amdgcn_s_memrealtime() - tstart < QDELAY) __builtin_amdgcn_s_sleep(1);
        if (wave > 8) __builtin_amdgcn_s_sleep(QSLEEP * 2);
        if (wave > 9) __builtin_amdgcn_s_sleep(QSLEEP * 2);
        const int off = GQ_OFF + (((j * 8 + (lane >> 3)) * 4) * 4 + ab) * 256 + (lane & 7) * 32;
        for (unsigned spins = 0;; ++spins) {
            CBAR();
            u32x4 pa[4], pb[4];
#pragma unroll
            for (int k = 0; k < 4; ++k) { pa[k] = __builtin_amdgcn_raw_buffer_load_b128(rs, off + k * 1024, 0, SC1); pb[k] = __builtin_amdgcn_raw_buffer_load_b128(rs, off + k * 1024 + 16, 0, SC1); }
            SB();
            if (spins == 0 && lane == 0) __hip_atomic_store(cnt + 6, 1u, __ATOMIC_RELAXED, __HIP_MEMORY_SCOPE_WORKGROUP);
            SB();
            u32 bad = 0;
#pragma unroll
            for (int k = 0; k < 4; ++k) bad |= pk24_bad(pa[k], tg) | pk24_bad(pb[k], tg);
            if (__all(bad == 0u) || spins > SPIN_CAP) {
                const f32x4 qa = (up24(pa[0]) + up24(pa[1])) + (up24(pa[2]) + up24(pa[3])), qb = (up24(pb[0]) + up24(pb[1])) + (up24(pb[2]) + up24(pb[3]));
                const u32x4 qv = {pack_bf16(qa[0], qa[1]), pack_bf16(qa[2], qa[3]), pack_bf16(qb[0], qb[1]), pack_bf16(qb[2], qb[3])};
                *reinterpret_cast<u32x4*>(smem + L_QS + lane * 16) = qv;
                LDS_FENCE();
                if (lane == 0) __hip_atomic_store(cnt + 3, 1u, __ATOMIC_RELAXED, __HIP_MEMORY_SCOPE_WORKGROUP);
                break;
            }
        }
        TS(3);
        TS_FLUSH();
    }
}

extern "C" void kernel_launch(void* const* d_in, const int* in_sizes, int n_in,
                              void* d_out, int out_size, void* d_ws, size_t ws_size,
                              hipStream_t stream) {
    fused_decode<<<256, NTHREADS, 0, stream>>>((const u16*)d_in[0], (const u16*)d_in[1], (const u16*)d_in[2],
                                               (const u16*)d_in[3], (const u16*)d_in[4], (const u16*)d_in[6],
                                               (const u16*)d_in[7], (const u16*)d_in[8], (const u16*)d_in[9],
                                               (const u16*)d_in[10], (u16*)d_out, (unsigned char*)d_ws);
}
```

```cpp
#include <hip/hip_runtime.h>
#include <hip/hip_bf16.h>
#include <stdint.h>

typedef unsigned short u16;
typedef unsigned int u32;
typedef u32 u32x2 __attribute__((ext_vector_type(2)));
typedef u32 u32x4 __attribute__((ext_vector_type(4)));
typedef float f32x2 __attribute__((ext_vector_type(2)));
typedef float f32x4 __attribute__((ext_vector_type(4)));
typedef short s16x4 __attribute__((ext_vector_type(4)));
typedef __bf16 bf16x8 __attribute__((ext_vector_type(8)));
typedef __bf16 bf16x2 __attribute__((ext_vector_type(2)));

#define NPOLL 1
#define WEARLY 4
#define QPRIO 0
#define VHOLD 1
#define FINW 2
#define KTRIG 1
#define VSPLIT 1
#define PDELAY 100
#define QDELAY 340
#define VDELAY 0
#define QSLEEP 4
#define SCHED 1
#define H2_DELAY 24
#define H3_DELAY 20
#define H2_SLEEP 2
#define H3_SLEEP 1
#define H2_DIRECT 16
constexpr int NTHREADS = 13 * 64;

constexpr int E_OFF = 0;
constexpr int GQ_OFF = 4096;
constexpr int GH_OFF = GQ_OFF + 262144;
constexpr int GO_OFF = GH_OFF + 131072;
constexpr int G3_OFF = GO_OFF + 1048576;
constexpr int WS_USED = G3_OFF + 4 * 96 * 64 * 32;
constexpr int OOB = 0x40000000;

constexpr int L_TP = 0;
constexpr int L_ST = L_TP + 8192;
constexpr int L_CNT = L_ST + 64;
constexpr int L_QS = L_CNT + 64;
constexpr int L_MX = L_QS + 1024;
constexpr int L_LS = L_MX + 128;
constexpr int L_OP = L_LS + 128;
constexpr int L_WP = L_OP + 16384;
constexpr int L_END = L_WP + 16384;
constexpr int LDS_BYTES = 84 * 1024;
static_assert(L_END <= LDS_BYTES, "lds");

constexpr unsigned SPIN_CAP = 1u << 18;
constexpr float LOG2E = 1.4426950408889634f;

__device__ __forceinline__ float bf_lo(u32 v) { return __uint_as_float(v << 16); }
__device__ __forceinline__ float bf_hi(u32 v) { return __uint_as_float(v & 0xffff0000u); }
__device__ __forceinline__ u32 pack_bf16(float lo, float hi) {
    bf16x2 v = {(__bf16)lo, (__bf16)hi};
    return __builtin_bit_cast(u32, v);
}
__device__ __forceinline__ float rbf(float f) { return bf_lo((u32)__builtin_bit_cast(u16, (__bf16)f)); }

template <int CTRL> __device__ __forceinline__ float dppf(float x) {
    return __builtin_bit_cast(float, __builtin_amdgcn_mov_dpp(__builtin_bit_cast(int, x), CTRL, 0xf, 0xf, true));
}
constexpr int XOR1 = 0xB1, XOR2 = 0x4E, ROR4 = 0x124, ROR8 = 0x128;
__device__ __forceinline__ float quad_sum(float x) { x += dppf<XOR1>(x); x += dppf<XOR2>(x); return x; }
__device__ __forceinline__ float quad_max(float x) { x = fmaxf(x, dppf<XOR1>(x)); x = fmaxf(x, dppf<XOR2>(x)); return x; }
__device__ __forceinline__ float ror_sum(float x) { x += dppf<ROR8>(x); x += dppf<ROR4>(x); return x; }
__device__ __forceinline__ float xrow_max(float x) {
    auto s = __builtin_amdgcn_permlane16_swap(__float_as_uint(x), __float_as_uint(x), false, false);
    x = fmaxf(__uint_as_float(s[0]), __uint_as_float(s[1]));
    auto t = __builtin_amdgcn_permlane32_swap(__float_as_uint(x), __float_as_uint(x), false, false);
    return fmaxf(__uint_as_float(t[0]), __uint_as_float(t[1]));
}
__device__ __forceinline__ float xrow_sum(float x) {
    auto s = __builtin_amdgcn_permlane16_swap(__float_as_uint(x), __float_as_uint(x), false, false);
    x = __uint_as_float(s[0]) + __uint_as_float(s[1]);
    auto t = __builtin_amdgcn_permlane32_swap(__float_as_uint(x), __float_as_uint(x), false, false);
    return __uint_as_float(t[0]) + __uint_as_float(t[1]);
}
__device__ __forceinline__ float wave_sum(float x) { return xrow_sum(ror_sum(quad_sum(x))); }

__device__ __forceinline__ u32 plo(u32 a, u32 b) { return __builtin_amdgcn_perm(b, a, 0x05040100u); }
__device__ __forceinline__ u32 phi(u32 a, u32 b) { return __builtin_amdgcn_perm(b, a, 0x07060302u); }

__device__ __forceinline__ f32x4 mfma16(u32x2 a, u32x2 b, f32x4 c) {
    return __builtin_amdgcn_mfma_f32_16x16x16bf16_1k(__builtin_bit_cast(s16x4, a), __builtin_bit_cast(s16x4, b), c, 0, 0, 0);
}
__device__ __forceinline__ f32x4 mfma32(u32x4 a, u32x4 b, f32x4 c) {
    return __builtin_amdgcn_mfma_f32_16x16x32_bf16(__builtin_bit_cast(bf16x8, a), __builtin_bit_cast(bf16x8, b), c, 0, 0, 0);
}
__device__ __forceinline__ float sel4(f32x4 d, int s) {
    float lo = (s & 1) ? d[1] : d[0], hi = (s & 1) ? d[3] : d[2];
    return (s & 2) ? hi : lo;
}
__device__ __forceinline__ void transpose4x8(const u32x4 (&w)[4], u32x2 (&f)[8]) {
#pragma unroll
    for (int r = 0; r < 4; ++r) {
        f[2 * r] = u32x2{plo(w[0][r], w[1][r]), plo(w[2][r], w[3][r])};
        f[2 * r + 1] = u32x2{phi(w[0][r], w[1][r]), phi(w[2][r], w[3][r])};
    }
}
struct Tag2 { u32 t1, t2; };
__device__ __forceinline__ Tag2 make_tag2(u32 tag) { return Tag2{tag & 0xffffu, ((tag * 40503u) ^ (tag >> 16)) & 0xffffu}; }
__device__ __forceinline__ u32x4 pk24(float a, float b, float c, float d, Tag2 t) {
    const u32 ua = __float_as_uint(a) + 0x80u, ub = __float_as_uint(b) + 0x80u, uc = __float_as_uint(c) + 0x80u, ud = __float_as_uint(d) + 0x80u;
    u32x4 r;
    r.x = __builtin_amdgcn_perm(ua, ub, 0x07060503u);
    r.y = (__builtin_amdgcn_perm(ub, ub, 0x02010c0cu)) | t.t1;
    r.z = __builtin_amdgcn_perm(uc, ud, 0x07060503u);
    r.w = (__builtin_amdgcn_perm(ud, ud, 0x02010c0cu)) | t.t2;
    return r;
}
__device__ __forceinline__ u32 pk24_bad(u32x4 u, Tag2 t) { return ((u.y ^ t.t1) | (u.w ^ t.t2)) & 0xffffu; }
__device__ __forceinline__ f32x4 up24(u32x4 u) {
    return f32x4{__uint_as_float(u.x & 0xffffff00u), __uint_as_float(__builtin_amdgcn_perm(u.x, u.y, 0x0403020cu)),
                 __uint_as_float(u.z & 0xffffff00u), __uint_as_float(__builtin_amdgcn_perm(u.z, u.w, 0x0403020cu))};
}
#define SC1 16
#define NT 2
#define CBAR() asm volatile("" ::: "memory")
#define KEEP4(v) asm volatile("" : "+v"((v).x), "+v"((v).y), "+v"((v).z), "+v"((v).w))
#define LDS_FENCE() asm volatile("s_waitcnt lgkmcnt(0)" ::: "memory")
#define LDS_LD(p) __hip_atomic_load((p), __ATOMIC_RELAXED, __HIP_MEMORY_SCOPE_WORKGROUP)
#define SB() __builtin_amdgcn_sched_barrier(0)

#define TS(k)
#define TS_DECL
#define TS_FLUSH()

__device__ __forceinline__ void tile_phase(unsigned char* smem, __amdgpu_buffer_rsrc_t rs, const u32x4 (&tw)[4], u32x2 af, int slot, int cidx, int goff, Tag2 tg, int lane) {
    const int g = lane >> 4, c = lane & 15;
    u32x2 f[8];
    transpose4x8(tw, f);
    const f32x4 z = {0.f, 0.f, 0.f, 0.f};
    float v[8];
#pragma unroll
    for (int e = 0; e < 8; ++e) { const f32x4 d = mfma16(af, f[e], z); const float sv = (c & 8) ? d[2] : d[0]; v[e] = sv + dppf<ROR8>(sv); }
    if (c < 8) {
        float* dst = reinterpret_cast<float*>(smem + L_TP) + (slot * 4 + g) * 64 + c * 8;
        *reinterpret_cast<f32x4*>(dst) = f32x4{v[0], v[1], v[2], v[3]};
        *reinterpret_cast<f32x4*>(dst + 4) = f32x4{v[4], v[5], v[6], v[7]};
    }
    LDS_FENCE();
    u32 old = 0;
    if (lane == 0) old = __hip_atomic_fetch_add(reinterpret_cast<u32*>(smem + L_CNT) + cidx, 1u, __ATOMIC_RELAXED, __HIP_MEMORY_SCOPE_WORKGROUP);
    old = __builtin_amdgcn_readfirstlane(old);
    if (old == 3u) {
        CBAR();
        const float* src = reinterpret_cast<const float*>(smem + L_TP) + (slot & 4) * 256 + lane * 4;
        f32x4 a = *reinterpret_cast<const f32x4*>(src);
#pragma unroll
        for (int w = 1; w < 4; ++w) a += *reinterpret_cast<const f32x4*>(src + w * 256);
        __builtin_amdgcn_raw_buffer_store_b128(pk24(a[0], a[1], a[2], a[3], tg), rs, goff + lane * 16, 0, SC1);
        SB();
        if (lane == 0) __hip_atomic_store(reinterpret_cast<u32*>(smem + L_CNT) + cidx, 5u, __ATOMIC_RELAXED, __HIP_MEMORY_SCOPE_WORKGROUP);
    }
}

__global__ void __launch_bounds__(NTHREADS, 4)
fused_decode(const u16* __restrict__ x, const u16* __restrict__ kc, const u16* __restrict__ vc,
             const u16* __restrict__ wq, const u16* __restrict__ wo, const u16* __restrict__ w1,
             const u16* __restrict__ b1, const u16* __restrict__ w2, const u16* __restrict__ b2,
             const u16* __restrict__ gw, u16* __restrict__ out, unsigned char* ws) {
    __shared__ __attribute__((aligned(16))) unsigned char smem[LDS_BYTES];
    const int tid = threadIdx.x, lane = tid & 63;
    const int wave = __builtin_amdgcn_readfirstlane(tid >> 6);
    const int bid = blockIdx.x, j = bid & 7, i = bid >> 3;
    const bool setA = i < 14, setB = (i >= 14) && (i < 28);
    const int g = lane >> 4, c = lane & 15;
    const __amdgpu_buffer_rsrc_t rs = __builtin_amdgcn_make_buffer_rsrc(ws, 0, WS_USED, 0x00020000);
    const int ab = i >> 3, as = i & 7;
    const int rg = 8 * j + (i >> 2), cgp = i & 3;
    const int tb = j * 14 + (i - 14), rg2 = tb >> 2, cg2 = tb & 3;
    u32* const cnt = reinterpret_cast<u32*>(smem + L_CNT);
    TS_DECL
    TS(0);
    const unsigned long long tstart = __builtin_amdgcn_s_memrealtime();

    if (wave < 8) {
        const bool lowh = wave < 4;
        const int w4 = wave & 3;
        const int r0 = 128 * (i >> 3) + 32 * w4;
        const __amdgpu_buffer_rsrc_t r1 = __builtin_amdgcn_make_buffer_rsrc((void*)wq, 0, 512 * 4096 * 2, 0x00020000);
        const int off1 = lowh ? (r0 + (g * 2 + (c >> 3)) * 4) * 8192 + (512 * j + 64 * (i & 7) + (c & 7) * 8) * 2 : OOB;
        u32x4 t1[4];
#pragma unroll
        for (int t = 0; t < 4; ++t) t1[t] = __builtin_amdgcn_raw_buffer_load_b128(r1, off1 + t * 8192, 0, NT);
        const u32x2 xa = *reinterpret_cast<const u32x2*>(x + (c >> 2) * 512 + r0 + (g * 2 + ((c >> 1) & 1)) * 4);
        const bool has2 = lowh || setB;
        const __amdgpu_buffer_rsrc_t r2 = __builtin_amdgcn_make_buffer_rsrc((void*)(lowh ? wo : w2), 0, lowh ? 4096 * 512 * 2 : 1792 * 512 * 2, 0x00020000);
        const int off2 = has2 ? ((64 * (lowh ? rg : rg2) + 16 * w4 + 4 * g) * 512 + 128 * (lowh ? cgp : cg2) + 8 * c) * 2 : OOB;
        u32x4 kf[4], vf[4], wf[4];
        const u32 ev = __builtin_amdgcn_raw_buffer_load_b32(rs, E_OFF, 0, SC1);
        const __amdgpu_buffer_rsrc_t rb1 = __builtin_amdgcn_make_buffer_rsrc((void*)b1, 0, 1792 * 2, 0x00020000);
        const u32x2 b1v = __builtin_amdgcn_raw_buffer_load_b64(rb1, (!lowh && setB) ? (64 * rg2 + 16 * w4 + 4 * g) * 2 : OOB, 0, 0);
        const u32 b2v = *reinterpret_cast<const u32*>(b2 + 64 * j + 2 * i);
        const __amdgpu_buffer_rsrc_t rk = __builtin_amdgcn_make_buffer_rsrc((void*)kc, 0, 32 << 20, 0x00020000);
        const __amdgpu_buffer_rsrc_t rv = __builtin_amdgcn_make_buffer_rsrc((void*)vc, 0, 32 << 20, 0x00020000);
        const int kvb = ((ab * 8 + j) * 4096 + 3072 + 128 * as + 16 * wave) * 256;
        const int offk = kvb + (4 * ((lane >> 2) & 3)) * 256 + (g * 4 + (lane & 3)) * 16;
        const int offv = kvb + (4 * g) * 256 + c * 16;
        SB();
#pragma unroll
        for (int t = 0; t < WEARLY; ++t) wf[t] = __builtin_amdgcn_raw_buffer_load_b128(r2, off2 + t * 1024, 0, NT);
        SB();
        __syncthreads();
        SB();
        const u32 tag = __builtin_amdgcn_readfirstlane(ev) + 1u;
        const Tag2 tg = make_tag2(tag);

        if (lowh) tile_phase(smem, rs, t1, xa, wave, 0, GQ_OFF + ((j * 8 + (i & 7)) * 4 + (i >> 3)) * 1024, tg, lane);
        TS(1);
        while (LDS_LD(cnt + 0) < 5u) __builtin_amdgcn_s_sleep(1);
        CBAR();
#pragma unroll
        for (int t = 0; t < 4; ++t) kf[t] = __builtin_amdgcn_raw_buffer_load_b128(rk, offk + t * 256, 0, NT);
        SB();
        SB();
        KEEP4(kf[0]); KEEP4(kf[1]);
        if (lane == 0) __hip_atomic_fetch_add(cnt + 5, 1u, __ATOMIC_RELAXED, __HIP_MEMORY_SCOPE_WORKGROUP);
        while (LDS_LD(cnt + 6) == 0u) __builtin_amdgcn_s_sleep(1);
        CBAR();
        vf[0] = __builtin_amdgcn_raw_buffer_load_b128(rv, offv, 0, NT); vf[1] = __builtin_amdgcn_raw_buffer_load_b128(rv, offv + 256, 0, NT);
        SB();
        KEEP4(kf[2]); KEEP4(kf[3]);
        while (LDS_LD(cnt + 6) < 2u) __builtin_amdgcn_s_sleep(1);
        CBAR();
        vf[2] = __builtin_amdgcn_raw_buffer_load_b128(rv, offv + 512, 0, NT); vf[3] = __builtin_amdgcn_raw_buffer_load_b128(rv, offv + 768, 0, NT);
        SB();
        TS(2);
        TS(3);
        while (LDS_LD(cnt + 3) == 0u) __builtin_amdgcn_s_sleep(1);
        CBAR();
        TS(4);
        float sc[4];
        {
            const u32x4 qf = *reinterpret_cast<const u32x4*>(smem + L_QS + (((lane >> 2) & 3) * 128 + (g * 4 + (lane & 3)) * 8) * 2);
            const f32x4 z = {0.f, 0.f, 0.f, 0.f};
#pragma unroll
            for (int t = 0; t < 4; ++t) {
                const f32x4 d = mfma32(kf[t], qf, z);
                sc[t] = rbf(rbf(quad_sum(sel4(d, lane & 3))) * 0.08838834764831845f);
            }
        }
        u32x2 pa;
        {
            const float M = xrow_max(fmaxf(fmaxf(sc[0], sc[1]), fmaxf(sc[2], sc[3])));
            const float p0 = __builtin_amdgcn_exp2f((sc[0] - M) * LOG2E), p1 = __builtin_amdgcn_exp2f((sc[1] - M) * LOG2E);
            const float p2 = __builtin_amdgcn_exp2f((sc[2] - M) * LOG2E), p3 = __builtin_amdgcn_exp2f((sc[3] - M) * LOG2E);
            pa = u32x2{pack_bf16(p0, p1), pack_bf16(p2, p3)};
            const float l = xrow_sum((p0 + p1) + (p2 + p3));
            if (g == 0 && (c & 3) == 0) { reinterpret_cast<float*>(smem + L_MX)[(c >> 2) * 8 + wave] = M; reinterpret_cast<float*>(smem + L_LS)[(c >> 2) * 8 + wave] = l; }
        }
        TS(5);
        {
            u32x2 f[8];
            transpose4x8(vf, f);
            const f32x4 z = {0.f, 0.f, 0.f, 0.f};
            float o8[8];
#pragma unroll
            for (int e = 0; e < 8; ++e) o8[e] = mfma16(pa, f[e], z)[0];
            float* dst = reinterpret_cast<float*>(smem + L_OP) + (wave * 4 + g) * 128 + c * 8;
            *reinterpret_cast<f32x4*>(dst) = f32x4{o8[0], o8[1], o8[2], o8[3]};
            *reinterpret_cast<f32x4*>(dst + 4) = f32x4{o8[4], o8[5], o8[6], o8[7]};
        }
        __syncthreads();
        if (wave >= 4) {
            const int hl = wave - 4, pc = lane & 7;
            const float* src = reinterpret_cast<const float*>(smem + L_OP) + hl * 128 + (lane >> 3) * 16 + (pc & 3) * 4;
            const f32x4 la = *reinterpret_cast<const f32x4*>(smem + L_LS + hl * 32), lb_ = *reinterpret_cast<const f32x4*>(smem + L_LS + hl * 32 + 16);
            const f32x4 ma = *reinterpret_cast<const f32x4*>(smem + L_MX + hl * 32), mb = *reinterpret_cast<const f32x4*>(smem + L_MX + hl * 32 + 16);
            const float Mh = fmaxf(fmaxf(fmaxf(ma[0], ma[1]), fmaxf(ma[2], ma[3])), fmaxf(fmaxf(mb[0], mb[1]), fmaxf(mb[2], mb[3])));
            f32x4 a = {0.f, 0.f, 0.f, 0.f};
            float L = 0.f;
#pragma unroll
            for (int w = 0; w < 8; ++w) {
                const float sw = __builtin_amdgcn_exp2f(((w < 4 ? ma[w & 3] : mb[w & 3]) - Mh) * LOG2E);
                a += sw * *reinterpret_cast<const f32x4*>(src + w * 512);
                L = fmaf(sw, w < 4 ? la[w & 3] : lb_[w & 3], L);
            }
            const u32x4 gd = pk24(a[0], a[1], a[2], a[3], tg);
            const u32x4 gv = {pc < 4 ? gd.x : __float_as_uint(Mh), pc < 4 ? gd.y : tag, pc < 4 ? gd.z : __float_as_uint(L), pc < 4 ? gd.w : tag};
            __builtin_amdgcn_raw_buffer_store_b128(gv, rs, pc <= 4 ? GO_OFF + ((((ab * 8 + j) * 8 + as) * 4 + hl) * 64 + lane) * 16 : OOB, 0, SC1);
            if (wave == 7) TS(11);
        }
        TS(6);
        if (has2) {
            u32x2 bfr[8];
            transpose4x8(wf, bfr);
#pragma unroll
            for (int e = 0; e < 8; ++e) asm volatile("" : "+v"(bfr[e].x), "+v"(bfr[e].y));
            u32x2 af;
            const int lb = c >> 2, sq = c & 3;
            if (lowh) {
                const int head = rg >> 1, hl = head & 3;
                const int offl = GO_OFF + ((((lb * 8 + j) * 8 + sq) * 4 + hl) * 8 + 4 * (rg & 1) + w4) * 128;
                u32x4 g0 = {0u, 0u, 0u, 0u}, g1 = g0, m0 = g0, m1 = g0;
                bool ok = false;
                __builtin_amdgcn_s_sleep(H2_DELAY);
                for (unsigned spins = 0;;) {
                    CBAR();
                    if (!ok) {
                        g0 = __builtin_amdgcn_raw_buffer_load_b128(rs, offl + g * 16, 0, SC1);
                        g1 = __builtin_amdgcn_raw_buffer_load_b128(rs, offl + g * 16 + 16384, 0, SC1);
                        m0 = __builtin_amdgcn_raw_buffer_load_b128(rs, offl + 64, 0, SC1);
                        m1 = __builtin_amdgcn_raw_buffer_load_b128(rs, offl + 64 + 16384, 0, SC1);
                        ok = ((pk24_bad(g0, tg) | pk24_bad(g1, tg)) | (((m0.y ^ tag) | (m0.w ^ tag)) | ((m1.y ^ tag) | (m1.w ^ tag)))) == 0u;
                    }
                    if (__all(ok) || ++spins > SPIN_CAP) break;
                    __builtin_amdgcn_s_sleep(H2_SLEEP);
                }
                const float ma = __uint_as_float(m0.x), mb = __uint_as_float(m1.x);
                const float MM = quad_max(fmaxf(ma, mb));
                const float w0 = __builtin_amdgcn_exp2f((ma - MM) * LOG2E), w1_ = __builtin_amdgcn_exp2f((mb - MM) * LOG2E);
                const float LL = quad_sum(fmaf(w0, __uint_as_float(m0.z), w1_ * __uint_as_float(m1.z)));
                const float inv = 1.0f / LL;
                const f32x4 oa = up24(g0), ob = up24(g1);
                const float v0 = quad_sum(fmaf(w0, oa[0], w1_ * ob[0])) * inv, v1 = quad_sum(fmaf(w0, oa[1], w1_ * ob[1])) * inv;
                const float v2 = quad_sum(fmaf(w0, oa[2], w1_ * ob[2])) * inv, v3 = quad_sum(fmaf(w0, oa[3], w1_ * ob[3])) * inv;
                af = u32x2{pack_bf16(v0, v1), pack_bf16(v2, v3)};
            } else {
                const int offh = GH_OFF + ((rg2 * 4 + sq) * 4 + lb) * 256 + (8 * w4 + 2 * g) * 8;
                u32x4 gv;
                for (unsigned spins = 0;;) {
                    CBAR();
                    gv = __builtin_amdgcn_raw_buffer_load_b128(rs, offh, 0, SC1);
                    if (__all(pk24_bad(gv, tg) == 0u) || ++spins > SPIN_CAP) break;
                    __builtin_amdgcn_s_sleep(H2_SLEEP);
                }
                const f32x4 hp = up24(gv);
                float zz[4] = {quad_sum(hp[0]), quad_sum(hp[1]), quad_sum(hp[2]), quad_sum(hp[3])};
                const float bb[4] = {bf_lo(b1v.x), bf_hi(b1v.x), bf_lo(b1v.y), bf_hi(b1v.y)};
#pragma unroll
                for (int e = 0; e < 4; ++e) {
                    const float z = rbf(rbf(zz[e]) + bb[e]);
                    const float u = 0.7978845608028654f * (z + 0.044715f * z * z * z);
                    const float th = 1.0f - 2.0f / (__expf(2.0f * u) + 1.0f);
                    zz[e] = 0.5f * z * (1.0f + th);
                }
                af = u32x2{pack_bf16(zz[0], zz[1]), pack_bf16(zz[2], zz[3])};
            }
            TS(7);
            const f32x4 z = {0.f, 0.f, 0.f, 0.f};
            float o8[8];
#pragma unroll
            for (int e = 0; e < 8; ++e) o8[e] = mfma16(af, bfr[e], z)[0];
            float* dst = reinterpret_cast<float*>(smem + L_WP) + (wave * 4 + g) * 128 + c * 8;
            *reinterpret_cast<f32x4*>(dst) = f32x4{o8[0], o8[1], o8[2], o8[3]};
            *reinterpret_cast<f32x4*>(dst + 4) = f32x4{o8[4], o8[5], o8[6], o8[7]};
        }
        __syncthreads();
        if (has2 && w4 < 2) {
            const int t7 = w4 * 64 + lane, r = t7 >> 1, u2 = t7 & 1, wb = wave & 4;
            f32x2 s0 = {0.f, 0.f}, s1 = {0.f, 0.f};
#pragma unroll
            for (int w = 0; w < 4; ++w) {
                s0 += *reinterpret_cast<const f32x2*>(smem + L_WP + (((wb + w) * 4 + 2 * u2) * 128 + 2 * r) * 4);
                s1 += *reinterpret_cast<const f32x2*>(smem + L_WP + (((wb + w) * 4 + 2 * u2 + 1) * 128 + 2 * r) * 4);
            }
            const int cgx = lowh ? cgp : cg2, slot3 = lowh ? rg : 64 + rg2;
            __builtin_amdgcn_raw_buffer_store_b128(pk24(s0.x, s0.y, s1.x, s1.y, tg), rs, G3_OFF + ((cgx * 96 + slot3) * 128 + t7) * 16, 0, SC1);
        }
        TS(8);
        if (wave == FINW) {
            const int cgf = j >> 1, rr = (j & 1) * 32 + i;
            const int pp = lane >> 1, u2 = lane & 1;
            const int e = ((lane >> 5) << 1) | ((lane >> 4) & 1), ob = 2 * u2 + (e >> 1), oc = e & 1;
            const float gt = reinterpret_cast<const float*>(smem + L_ST)[ob * 4 + 3];
            const float b2f = oc ? bf_hi(b2v) : bf_lo(b2v);
            const float omg = rbf(1.0f - gt);
            const int off = G3_OFF + (((cgf * 96 + pp) * 64 + rr) * 2 + u2) * 16;
            u32x4 gv[3];
#pragma unroll
            for (int u = 0; u < 3; ++u) gv[u] = u32x4{0u, 0u, 0u, 0u};
            bool ok = false;
            __builtin_amdgcn_s_sleep(H3_DELAY);
            for (unsigned spins = 0;;) {
                CBAR();
                if (!ok) {
#pragma unroll
                    for (int u = 0; u < 3; ++u) gv[u] = __builtin_amdgcn_raw_buffer_load_b128(rs, off + u * (32 * 2048), 0, SC1);
                    u32 bad = pk24_bad(gv[0], tg) | pk24_bad(gv[1], tg);
                    if (pp < 28) bad |= pk24_bad(gv[2], tg);
                    ok = bad == 0u;
                }
                if (__all(ok) || ++spins > SPIN_CAP) break;
                __builtin_amdgcn_s_sleep(H3_SLEEP);
            }
            const f32x4 av = up24(gv[0]) + up24(gv[1]);
            f32x4 cv = up24(gv[2]);
            if (pp >= 28) cv = f32x4{0.f, 0.f, 0.f, 0.f};
            const bool isc = (lane & 2) != 0;
            float v[4];
#pragma unroll
            for (int k = 0; k < 4; ++k) { const float keep = isc ? cv[k] : av[k], give = isc ? av[k] : cv[k]; v[k] = keep + dppf<XOR2>(give); }
            auto sw0 = __builtin_amdgcn_permlane32_swap(__float_as_uint(v[0]), __float_as_uint(v[2]), false, false);
            auto sw1 = __builtin_amdgcn_permlane32_swap(__float_as_uint(v[1]), __float_as_uint(v[3]), false, false);
            const float x0 = __uint_as_float(sw0[0]) + __uint_as_float(sw0[1]), x1 = __uint_as_float(sw1[0]) + __uint_as_float(sw1[1]);
            auto sw2 = __builtin_amdgcn_permlane16_swap(__float_as_uint(x0), __float_as_uint(x1), false, false);
            float y = __uint_as_float(sw2[0]) + __uint_as_float(sw2[1]);
            y = ror_sum(y);
            const float other = dppf<XOR2>(y);
            const float res = rbf(gt * rbf(y)) + rbf(omg * rbf(rbf(other) + b2f));
            if ((lane & 14) == 0) out[ob * 512 + 64 * j + 2 * i + oc] = __builtin_bit_cast(u16, (__bf16)res);
            if (bid == 0 && lane == 0) __builtin_amdgcn_raw_buffer_store_b32(tag, rs, E_OFF, 0, SC1);
            TS(9);
        }
        TS_FLUSH();
    } else {
        const u32 ev = __builtin_amdgcn_raw_buffer_load_b32(rs, E_OFF, 0, SC1);
        if (wave > 8) {
            const int aw = wave - 9, ta = j * 14 + i, kq = ta & 3, cb = ta >> 2, r0 = 128 * kq + 32 * aw;
            const u32x4 xrow = *reinterpret_cast<const u32x4*>(x + aw * 512 + lane * 8);
            const u32x4 gwv = *reinterpret_cast<const u32x4*>(gw + lane * 8);
            const u32x2 xa = *reinterpret_cast<const u32x2*>(x + (c >> 2) * 512 + r0 + (g * 2 + ((c >> 1) & 1)) * 4);
            const __amdgpu_buffer_rsrc_t r1 = __builtin_amdgcn_make_buffer_rsrc((void*)w1, 0, 512 * 1792 * 2, 0x00020000);
            const int off1 = setA ? (r0 + (g * 2 + (c >> 3)) * 4) * 3584 + (64 * cb + (c & 7) * 8) * 2 : OOB;
            u32x4 t2[4];
#pragma unroll
            for (int t = 0; t < 4; ++t) t2[t] = __builtin_amdgcn_raw_buffer_load_b128(r1, off1 + t * 3584, 0, NT);
            SB();
            __syncthreads();
            SB();
            float f[8];
#pragma unroll
            for (int e = 0; e < 4; ++e) { f[2 * e] = bf_lo(xrow[e]); f[2 * e + 1] = bf_hi(xrow[e]); }
            const float mean = wave_sum(((f[0] + f[1]) + (f[2] + f[3])) + ((f[4] + f[5]) + (f[6] + f[7]))) * (1.0f / 512.0f);
            float vs = 0.f, gsum = 0.f;
#pragma unroll
            for (int e = 0; e < 4; ++e) {
                const float d0 = f[2 * e] - mean, d1 = f[2 * e + 1] - mean; vs = fmaf(d0, d0, vs); vs = fmaf(d1, d1, vs);
                gsum += rbf(f[2 * e] * bf_lo(gwv[e])); gsum += rbf(f[2 * e + 1] * bf_hi(gwv[e]));
            }
            const float var = wave_sum(vs) * (1.0f / 512.0f);
            const float gg = rbf(wave_sum(gsum));
            const float sd = rbf(sqrtf(rbf(rbf(var) + 1e-5f)));
            if (lane == 0) *reinterpret_cast<f32x4*>(smem + L_ST + aw * 16) = f32x4{rbf(mean), sd, 1.0f / sd, rbf(1.0f / (1.0f + __expf(-gg)))};
            LDS_FENCE();
            if (lane == 0) __hip_atomic_fetch_add(cnt + 2, 1u, __ATOMIC_RELAXED, __HIP_MEMORY_SCOPE_WORKGROUP);
            if (setA) {
                while (LDS_LD(cnt + 2) < 4u) __builtin_amdgcn_s_sleep(1);
                CBAR();
                const f32x4 stt = *reinterpret_cast<const f32x4*>(smem + L_ST + (c >> 2) * 16);
                float d[4] = {rbf(bf_lo(xa.x) - stt[0]), rbf(bf_hi(xa.x) - stt[0]), rbf(bf_lo(xa.y) - stt[0]), rbf(bf_hi(xa.y) - stt[0])};
#pragma unroll
                for (int e = 0; e < 4; ++e) { const float q0 = d[e] * stt[2]; d[e] = fmaf(fmaf(-q0, stt[1], d[e]), stt[2], q0); }
                const u32 tag = __builtin_amdgcn_readfirstlane(ev) + 1u;
                tile_phase(smem, rs, t2, u32x2{pack_bf16(d[0], d[1]), pack_bf16(d[2], d[3])}, 4 + aw, 1, GH_OFF + (cb * 4 + kq) * 1024, make_tag2(tag), lane);
            }
            return;
        }
        if (lane < 8) cnt[lane] = 0u;
        SB();
        __syncthreads();
        SB();
        TS(1);
        const u32 tag = __builtin_amdgcn_readfirstlane(ev) + 1u;
        const Tag2 tg = make_tag2(tag);
        while (LDS_LD(cnt + 0) < 5u) __builtin_amdgcn_s_sleep(1);
        const unsigned long long tpub = __builtin_amdgcn_s_memrealtime();
        while (LDS_LD(cnt + 5) < KTRIG) __builtin_amdgcn_s_sleep(1);
        while (__builtin_amdgcn_s_memrealtime() - tstart < QDELAY || __builtin_amdgcn_s_memrealtime() - tpub < PDELAY) __builtin_amdgcn_s_sleep(1);
        const int off = GQ_OFF + (((j * 8 + (lane >> 3)) * 4) * 4 + ab) * 256 + (lane & 7) * 32;
        for (unsigned spins = 0;; ++spins) {
            CBAR();
            u32x4 pa[4], pb[4];
#pragma unroll
            for (int k = 0; k < 4; ++k) { pa[k] = __builtin_amdgcn_raw_buffer_load_b128(rs, off + k * 1024, 0, SC1); pb[k] = __builtin_amdgcn_raw_buffer_load_b128(rs, off + k * 1024 + 16, 0, SC1); }
            SB();
            if (spins < 2 && lane == 0) __hip_atomic_store(cnt + 6, spins + 1u, __ATOMIC_RELAXED, __HIP_MEMORY_SCOPE_WORKGROUP);
            SB();
            u32 bad = 0;
#pragma unroll
            for (int k = 0; k < 4; ++k) bad |= pk24_bad(pa[k], tg) | pk24_bad(pb[k], tg);
            if (__all(bad == 0u) || spins > SPIN_CAP) {
                const f32x4 qa = (up24(pa[0]) + up24(pa[1])) + (up24(pa[2]) + up24(pa[3])), qb = (up24(pb[0]) + up24(pb[1])) + (up24(pb[2]) + up24(pb[3]));
                const u32x4 qv = {pack_bf16(qa[0], qa[1]), pack_bf16(qa[2], qa[3]), pack_bf16(qb[0], qb[1]), pack_bf16(qb[2], qb[3])};
                *reinterpret_cast<u32x4*>(smem + L_QS + lane * 16) = qv;
                LDS_FENCE();
                if (lane == 0) { __hip_atomic_store(cnt + 3, 1u, __ATOMIC_RELAXED, __HIP_MEMORY_SCOPE_WORKGROUP); __hip_atomic_store(cnt + 6, 2u, __ATOMIC_RELAXED, __HIP_MEMORY_SCOPE_WORKGROUP); }
                break;
            }
        }
        TS(3);
        TS_FLUSH();
    }
}

extern "C" void kernel_launch(void* const* d_in, const int* in_sizes, int n_in,
                              void* d_out, int out_size, void* d_ws, size_t ws_size,
                              hipStream_t stream) {
    fused_decode<<<256, NTHREADS, 0, stream>>>((const u16*)d_in[0], (const u16*)d_in[1], (const u16*)d_in[2],
                                               (const u16*)d_in[3], (const u16*)d_in[4], (const u16*)d_in[6],
                                               (const u16*)d_in[7], (const u16*)d_in[8], (const u16*)d_in[9],
                                               (const u16*)d_in[10], (u16*)d_out, (unsigned char*)d_ws);
}
```

```cpp
#include <hip/hip_runtime.h>
#include <hip/hip_bf16.h>
#include <stdint.h>

typedef unsigned short u16;
typedef unsigned int u32;
typedef u32 u32x2 __attribute__((ext_vector_type(2)));
typedef u32 u32x4 __attribute__((ext_vector_type(4)));
typedef float f32x2 __attribute__((ext_vector_type(2)));
typedef float f32x4 __attribute__((ext_vector_type(4)));
typedef short s16x4 __attribute__((ext_vector_type(4)));
typedef __bf16 bf16x8 __attribute__((ext_vector_type(8)));
typedef __bf16 bf16x2 __attribute__((ext_vector_type(2)));

constexpr unsigned long long QDELAY = 340, PDELAY = 100;
constexpr int H2_DELAY = 24, H3_DELAY = 20, H2_SLEEP = 2, H3_SLEEP = 1;
constexpr int FINW = 2;
constexpr int NTHREADS = 13 * 64;

constexpr int E_OFF = 0;
constexpr int GQ_OFF = 4096;
constexpr int GH_OFF = GQ_OFF + 262144;
constexpr int GO_OFF = GH_OFF + 131072;
constexpr int G3_OFF = GO_OFF + 1048576;
constexpr int WS_USED = G3_OFF + 4 * 96 * 64 * 32;
constexpr int OOB = 0x40000000;

constexpr int L_TP = 0;
constexpr int L_ST = L_TP + 8192;
constexpr int L_CNT = L_ST + 64;
constexpr int L_QS = L_CNT + 64;
constexpr int L_MX = L_QS + 1024;
constexpr int L_LS = L_MX + 128;
constexpr int L_OP = L_LS + 128;
constexpr int L_WP = L_OP + 16384;
constexpr int L_END = L_WP + 16384;
constexpr int LDS_BYTES = 84 * 1024;
static_assert(L_END <= LDS_BYTES, "lds");

constexpr unsigned SPIN_CAP = 1u << 18;
constexpr float LOG2E = 1.4426950408889634f;

__device__ __forceinline__ float bf_lo(u32 v) { return __uint_as_float(v << 16); }
__device__ __forceinline__ float bf_hi(u32 v) { return __uint_as_float(v & 0xffff0000u); }
__device__ __forceinline__ u32 pack_bf16(float lo, float hi) {
    bf16x2 v = {(__bf16)lo, (__bf16)hi};
    return __builtin_bit_cast(u32, v);
}
__device__ __forceinline__ float rbf(float f) { return bf_lo((u32)__builtin_bit_cast(u16, (__bf16)f)); }

template <int CTRL> __device__ __forceinline__ float dppf(float x) {
    return __builtin_bit_cast(float, __builtin_amdgcn_mov_dpp(__builtin_bit_cast(int, x), CTRL, 0xf, 0xf, true));
}
constexpr int XOR1 = 0xB1, XOR2 = 0x4E, ROR4 = 0x124, ROR8 = 0x128;
__device__ __forceinline__ float quad_sum(float x) { x += dppf<XOR1>(x); x += dppf<XOR2>(x); return x; }
__device__ __forceinline__ float quad_max(float x) { x = fmaxf(x, dppf<XOR1>(x)); x = fmaxf(x, dppf<XOR2>(x)); return x; }
__device__ __forceinline__ float ror_sum(float x) { x += dppf<ROR8>(x); x += dppf<ROR4>(x); return x; }
__device__ __forceinline__ float xrow_max(float x) {
    auto s = __builtin_amdgcn_permlane16_swap(__float_as_uint(x), __float_as_uint(x), false, false);
    x = fmaxf(__uint_as_float(s[0]), __uint_as_float(s[1]));
    auto t = __builtin_amdgcn_permlane32_swap(__float_as_uint(x), __float_as_uint(x), false, false);
    return fmaxf(__uint_as_float(t[0]), __uint_as_float(t[1]));
}
__device__ __forceinline__ float xrow_sum(float x) {
    auto s = __builtin_amdgcn_permlane16_swap(__float_as_uint(x), __float_as_uint(x), false, false);
    x = __uint_as_float(s[0]) + __uint_as_float(s[1]);
    auto t = __builtin_amdgcn_permlane32_swap(__float_as_uint(x), __float_as_uint(x), false, false);
    return __uint_as_float(t[0]) + __uint_as_float(t[1]);
}
__device__ __forceinline__ float wave_sum(float x) { return xrow_sum(ror_sum(quad_sum(x))); }

__device__ __forceinline__ u32 plo(u32 a, u32 b) { return __builtin_amdgcn_perm(b, a, 0x05040100u); }
__device__ __forceinline__ u32 phi(u32 a, u32 b) { return __builtin_amdgcn_perm(b, a, 0x07060302u); }

__device__ __forceinline__ f32x4 mfma16(u32x2 a, u32x2 b, f32x4 c) {
    return __builtin_amdgcn_mfma_f32_16x16x16bf16_1k(__builtin_bit_cast(s16x4, a), __builtin_bit_cast(s16x4, b), c, 0, 0, 0);
}
__device__ __forceinline__ f32x4 mfma32(u32x4 a, u32x4 b, f32x4 c) {
    return __builtin_amdgcn_mfma_f32_16x16x32_bf16(__builtin_bit_cast(bf16x8, a), __builtin_bit_cast(bf16x8, b), c, 0, 0, 0);
}
__device__ __forceinline__ float sel4(f32x4 d, int s) {
    float lo = (s & 1) ? d[1] : d[0], hi = (s & 1) ? d[3] : d[2];
    return (s & 2) ? hi : lo;
}
__device__ __forceinline__ void transpose4x8(const u32x4 (&w)[4], u32x2 (&f)[8]) {
#pragma unroll
    for (int r = 0; r < 4; ++r) {
        f[2 * r] = u32x2{plo(w[0][r], w[1][r]), plo(w[2][r], w[3][r])};
        f[2 * r + 1] = u32x2{phi(w[0][r], w[1][r]), phi(w[2][r], w[3][r])};
    }
}
struct Tag2 { u32 t1, t2; };
__device__ __forceinline__ Tag2 make_tag2(u32 tag) { return Tag2{tag & 0xffffu, ((tag * 40503u) ^ (tag >> 16)) & 0xffffu}; }
__device__ __forceinline__ u32x4 pk24(float a, float b, float c, float d, Tag2 t) {
    const u32 ua = __float_as_uint(a) + 0x80u, ub = __float_as_uint(b) + 0x80u, uc = __float_as_uint(c) + 0x80u, ud = __float_as_uint(d) + 0x80u;
    u32x4 r;
    r.x = __builtin_amdgcn_perm(ua, ub, 0x07060503u);
    r.y = (__builtin_amdgcn_perm(ub, ub, 0x02010c0cu)) | t.t1;
    r.z = __builtin_amdgcn_perm(uc, ud, 0x07060503u);
    r.w = (__builtin_amdgcn_perm(ud, ud, 0x02010c0cu)) | t.t2;
    return r;
}
__device__ __forceinline__ u32 pk24_bad(u32x4 u, Tag2 t) { return ((u.y ^ t.t1) | (u.w ^ t.t2)) & 0xffffu; }
__device__ __forceinline__ f32x4 up24(u32x4 u) {
    return f32x4{__uint_as_float(u.x & 0xffffff00u), __uint_as_float(__builtin_amdgcn_perm(u.x, u.y, 0x0403020cu)),
                 __uint_as_float(u.z & 0xffffff00u), __uint_as_float(__builtin_amdgcn_perm(u.z, u.w, 0x0403020cu))};
}
#define SC1 16
#define NT 2
#define CBAR() asm volatile("" ::: "memory")
#define KEEP4(v) asm volatile("" : "+v"((v).x), "+v"((v).y), "+v"((v).z), "+v"((v).w))
#define LDS_FENCE() asm volatile("s_waitcnt lgkmcnt(0)" ::: "memory")
#define LDS_LD(p) __hip_atomic_load((p), __ATOMIC_RELAXED, __HIP_MEMORY_SCOPE_WORKGROUP)
#define SB() __builtin_amdgcn_sched_barrier(0)

__device__ __forceinline__ void tile_phase(unsigned char* smem, __amdgpu_buffer_rsrc_t rs, const u32x4 (&tw)[4], u32x2 af, int slot, int cidx, int goff, Tag2 tg, int lane) {
    const int g = lane >> 4, c = lane & 15;
    u32x2 f[8];
    transpose4x8(tw, f);
    const f32x4 z = {0.f, 0.f, 0.f, 0.f};
    float v[8];
#pragma unroll
    for (int e = 0; e < 8; ++e) { const f32x4 d = mfma16(af, f[e], z); const float sv = (c & 8) ? d[2] : d[0]; v[e] = sv + dppf<ROR8>(sv); }
    if (c < 8) {
        float* dst = reinterpret_cast<float*>(smem + L_TP) + (slot * 4 + g) * 64 + c * 8;
        *reinterpret_cast<f32x4*>(dst) = f32x4{v[0], v[1], v[2], v[3]};
        *reinterpret_cast<f32x4*>(dst + 4) = f32x4{v[4], v[5], v[6], v[7]};
    }
    LDS_FENCE();
    u32 old = 0;
    if (lane == 0) old = __hip_atomic_fetch_add(reinterpret_cast<u32*>(smem + L_CNT) + cidx, 1u, __ATOMIC_RELAXED, __HIP_MEMORY_SCOPE_WORKGROUP);
    old = __builtin_amdgcn_readfirstlane(old);
    if (old == 3u) {
        CBAR();
        const float* src = reinterpret_cast<const float*>(smem + L_TP) + (slot & 4) * 256 + lane * 4;
        f32x4 a = *reinterpret_cast<const f32x4*>(src);
#pragma unroll
        for (int w = 1; w < 4; ++w) a += *reinterpret_cast<const f32x4*>(src + w * 256);
        __builtin_amdgcn_raw_buffer_store_b128(pk24(a[0], a[1], a[2], a[3], tg), rs, goff + lane * 16, 0, SC1);
        SB();
        if (lane == 0) __hip_atomic_store(reinterpret_cast<u32*>(smem + L_CNT) + cidx, 5u, __ATOMIC_RELAXED, __HIP_MEMORY_SCOPE_WORKGROUP);
    }
}

__global__ void __launch_bounds__(NTHREADS, 4)
fused_decode(const u16* __restrict__ x, const u16* __restrict__ kc, const u16* __restrict__ vc,
             const u16* __restrict__ wq, const u16* __restrict__ wo, const u16* __restrict__ w1,
             const u16* __restrict__ b1, const u16* __restrict__ w2, const u16* __restrict__ b2,
             const u16* __restrict__ gw, u16* __restrict__ out, unsigned char* ws) {
    __shared__ __attribute__((aligned(16))) unsigned char smem[LDS_BYTES];
    const int tid = threadIdx.x, lane = tid & 63;
    const int wave = __builtin_amdgcn_readfirstlane(tid >> 6);
    const int bid = blockIdx.x, j = bid & 7, i = bid >> 3;
    const bool setA = i < 14, setB = (i >= 14) && (i < 28);
    const int g = lane >> 4, c = lane & 15;
    const __amdgpu_buffer_rsrc_t rs = __builtin_amdgcn_make_buffer_rsrc(ws, 0, WS_USED, 0x00020000);
    const int ab = i >> 3, as = i & 7;
    const int rg = 8 * j + (i >> 2), cgp = i & 3;
    const int tb = j * 14 + (i - 14), rg2 = tb >> 2, cg2 = tb & 3;
    u32* const cnt = reinterpret_cast<u32*>(smem + L_CNT);
    const unsigned long long tstart = __builtin_amdgcn_s_memrealtime();

    if (wave < 8) {
        const bool lowh = wave < 4;
        const int w4 = wave & 3;
        const int r0 = 128 * (i >> 3) + 32 * w4;
        const __amdgpu_buffer_rsrc_t r1 = __builtin_amdgcn_make_buffer_rsrc((void*)wq, 0, 512 * 4096 * 2, 0x00020000);
        const int off1 = lowh ? (r0 + (g * 2 + (c >> 3)) * 4) * 8192 + (512 * j + 64 * (i & 7) + (c & 7) * 8) * 2 : OOB;
        u32x4 t1[4];
#pragma unroll
        for (int t = 0; t < 4; ++t) t1[t] = __builtin_amdgcn_raw_buffer_load_b128(r1, off1 + t * 8192, 0, NT);
        const u32x2 xa = *reinterpret_cast<const u32x2*>(x + (c >> 2) * 512 + r0 + (g * 2 + ((c >> 1) & 1)) * 4);
        const bool has2 = lowh || setB;
        const __amdgpu_buffer_rsrc_t r2 = __builtin_amdgcn_make_buffer_rsrc((void*)(lowh ? wo : w2), 0, lowh ? 4096 * 512 * 2 : 1792 * 512 * 2, 0x00020000);
        const int off2 = has2 ? ((64 * (lowh ? rg : rg2) + 16 * w4 + 4 * g) * 512 + 128 * (lowh ? cgp : cg2) + 8 * c) * 2 : OOB;
        u32x4 kf[4], vf[4], wf[4];
        const u32 ev = __builtin_amdgcn_raw_buffer_load_b32(rs, E_OFF, 0, SC1);
        const __amdgpu_buffer_rsrc_t rb1 = __builtin_amdgcn_make_buffer_rsrc((void*)b1, 0, 1792 * 2, 0x00020000);
        const u32x2 b1v = __builtin_amdgcn_raw_buffer_load_b64(rb1, (!lowh && setB) ? (64 * rg2 + 16 * w4 + 4 * g) * 2 : OOB, 0, 0);
        const u32 b2v = *reinterpret_cast<const u32*>(b2 + 64 * j + 2 * i);
        const __amdgpu_buffer_rsrc_t rk = __builtin_amdgcn_make_buffer_rsrc((void*)kc, 0, 32 << 20, 0x00020000);
        const __amdgpu_buffer_rsrc_t rv = __builtin_amdgcn_make_buffer_rsrc((void*)vc, 0, 32 << 20, 0x00020000);
        const int kvb = ((ab * 8 + j) * 4096 + 3072 + 128 * as + 16 * wave) * 256;
        const int offk = kvb + (4 * ((lane >> 2) & 3)) * 256 + (g * 4 + (lane & 3)) * 16;
        const int offv = kvb + (4 * g) * 256 + c * 16;
        SB();
#pragma unroll
        for (int t = 0; t < 4; ++t) wf[t] = __builtin_amdgcn_raw_buffer_load_b128(r2, off2 + t * 1024, 0, NT);
        SB();
        __syncthreads();
        SB();
        const u32 tag = __builtin_amdgcn_readfirstlane(ev) + 1u;
        const Tag2 tg = make_tag2(tag);

        if (lowh) tile_phase(smem, rs, t1, xa, wave, 0, GQ_OFF + ((j * 8 + (i & 7)) * 4 + (i >> 3)) * 1024, tg, lane);
        while (LDS_LD(cnt + 0) < 5u) __builtin_amdgcn_s_sleep(1);
        CBAR();
#pragma unroll
        for (int t = 0; t < 4; ++t) kf[t] = __builtin_amdgcn_raw_buffer_load_b128(rk, offk + t * 256, 0, NT);
        SB();
        SB();
        KEEP4(kf[0]); KEEP4(kf[1]);
        if (lane == 0) __hip_atomic_fetch_add(cnt + 5, 1u, __ATOMIC_RELAXED, __HIP_MEMORY_SCOPE_WORKGROUP);
        while (LDS_LD(cnt + 6) == 0u) __builtin_amdgcn_s_sleep(1);
        CBAR();
        vf[0] = __builtin_amdgcn_raw_buffer_load_b128(rv, offv, 0, NT); vf[1] = __builtin_amdgcn_raw_buffer_load_b128(rv, offv + 256, 0, NT);
        SB();
        KEEP4(kf[2]); KEEP4(kf[3]);
        while (LDS_LD(cnt + 6) < 2u) __builtin_amdgcn_s_sleep(1);
        CBAR();
        vf[2] = __builtin_amdgcn_raw_buffer_load_b128(rv, offv + 512, 0, NT); vf[3] = __builtin_amdgcn_raw_buffer_load_b128(rv, offv + 768, 0, NT);
        SB();
        while (LDS_LD(cnt + 3) == 0u) __builtin_amdgcn_s_sleep(1);
        CBAR();
        float sc[4];
        {
            const u32x4 qf = *reinterpret_cast<const u32x4*>(smem + L_QS + (((lane >> 2) & 3) * 128 + (g * 4 + (lane & 3)) * 8) * 2);
            const f32x4 z = {0.f, 0.f, 0.f, 0.f};
#pragma unroll
            for (int t = 0; t < 4; ++t) {
                const f32x4 d = mfma32(kf[t], qf, z);
                sc[t] = rbf(rbf(quad_sum(sel4(d, lane & 3))) * 0.08838834764831845f);
            }
        }
        u32x2 pa;
        {
            const float M = xrow_max(fmaxf(fmaxf(sc[0], sc[1]), fmaxf(sc[2], sc[3])));
            const float p0 = __builtin_amdgcn_exp2f((sc[0] - M) * LOG2E), p1 = __builtin_amdgcn_exp2f((sc[1] - M) * LOG2E);
            const float p2 = __builtin_amdgcn_exp2f((sc[2] - M) * LOG2E), p3 = __builtin_amdgcn_exp2f((sc[3] - M) * LOG2E);
            pa = u32x2{pack_bf16(p0, p1), pack_bf16(p2, p3)};
            const float l = xrow_sum((p0 + p1) + (p2 + p3));
            if (g == 0 && (c & 3) == 0) { reinterpret_cast<float*>(smem + L_MX)[(c >> 2) * 8 + wave] = M; reinterpret_cast<float*>(smem + L_LS)[(c >> 2) * 8 + wave] = l; }
        }
        {
            u32x2 f[8];
            transpose4x8(vf, f);
            const f32x4 z = {0.f, 0.f, 0.f, 0.f};
            float o8[8];
#pragma unroll
            for (int e = 0; e < 8; ++e) o8[e] = mfma16(pa, f[e], z)[0];
            float* dst = reinterpret_cast<float*>(smem + L_OP) + (wave * 4 + g) * 128 + c * 8;
            *reinterpret_cast<f32x4*>(dst) = f32x4{o8[0], o8[1], o8[2], o8[3]};
            *reinterpret_cast<f32x4*>(dst + 4) = f32x4{o8[4], o8[5], o8[6], o8[7]};
        }
        __syncthreads();
        if (wave >= 4) {
            const int hl = wave - 4, pc = lane & 7;
            const float* src = reinterpret_cast<const float*>(smem + L_OP) + hl * 128 + (lane >> 3) * 16 + (pc & 3) * 4;
            const f32x4 la = *reinterpret_cast<const f32x4*>(smem + L_LS + hl * 32), lb_ = *reinterpret_cast<const f32x4*>(smem + L_LS + hl * 32 + 16);
            const f32x4 ma = *reinterpret_cast<const f32x4*>(smem + L_MX + hl * 32), mb = *reinterpret_cast<const f32x4*>(smem + L_MX + hl * 32 + 16);
            const float Mh = fmaxf(fmaxf(fmaxf(ma[0], ma[1]), fmaxf(ma[2], ma[3])), fmaxf(fmaxf(mb[0], mb[1]), fmaxf(mb[2], mb[3])));
            f32x4 a = {0.f, 0.f, 0.f, 0.f};
            float L = 0.f;
#pragma unroll
            for (int w = 0; w < 8; ++w) {
                const float sw = __builtin_amdgcn_exp2f(((w < 4 ? ma[w & 3] : mb[w & 3]) - Mh) * LOG2E);
                a += sw * *reinterpret_cast<const f32x4*>(src + w * 512);
                L = fmaf(sw, w < 4 ? la[w & 3] : lb_[w & 3], L);
            }
            const u32x4 gd = pk24(a[0], a[1], a[2], a[3], tg);
            const u32x4 gv = {pc < 4 ? gd.x : __float_as_uint(Mh), pc < 4 ? gd.y : tag, pc < 4 ? gd.z : __float_as_uint(L), pc < 4 ? gd.w : tag};
            __builtin_amdgcn_raw_buffer_store_b128(gv, rs, pc <= 4 ? GO_OFF + ((((ab * 8 + j) * 8 + as) * 4 + hl) * 64 + lane) * 16 : OOB, 0, SC1);
        }
        if (has2) {
            u32x2 bfr[8];
            transpose4x8(wf, bfr);
#pragma unroll
            for (int e = 0; e < 8; ++e) asm volatile("" : "+v"(bfr[e].x), "+v"(bfr[e].y));
            u32x2 af;
            const int lb = c >> 2, sq = c & 3;
            if (lowh) {
                const int head = rg >> 1, hl = head & 3;
                const int offl = GO_OFF + ((((lb * 8 + j) * 8 + sq) * 4 + hl) * 8 + 4 * (rg & 1) + w4) * 128;
                u32x4 g0 = {0u, 0u, 0u, 0u}, g1 = g0, m0 = g0, m1 = g0;
                bool ok = false;
                __builtin_amdgcn_s_sleep(H2_DELAY);
                for (unsigned spins = 0;;) {
                    CBAR();
                    if (!ok) {
                        g0 = __builtin_amdgcn_raw_buffer_load_b128(rs, offl + g * 16, 0, SC1);
                        g1 = __builtin_amdgcn_raw_buffer_load_b128(rs, offl + g * 16 + 16384, 0, SC1);
                        m0 = __builtin_amdgcn_raw_buffer_load_b128(rs, offl + 64, 0, SC1);
                        m1 = __builtin_amdgcn_raw_buffer_load_b128(rs, offl + 64 + 16384, 0, SC1);
                        ok = ((pk24_bad(g0, tg) | pk24_bad(g1, tg)) | (((m0.y ^ tag) | (m0.w ^ tag)) | ((m1.y ^ tag) | (m1.w ^ tag)))) == 0u;
                    }
                    if (__all(ok) || ++spins > SPIN_CAP) break;
                    __builtin_amdgcn_s_sleep(H2_SLEEP);
                }
                const float ma = __uint_as_float(m0.x), mb = __uint_as_float(m1.x);
                const float MM = quad_max(fmaxf(ma, mb));
                const float w0 = __builtin_amdgcn_exp2f((ma - MM) * LOG2E), w1_ = __builtin_amdgcn_exp2f((mb - MM) * LOG2E);
                const float LL = quad_sum(fmaf(w0, __uint_as_float(m0.z), w1_ * __uint_as_float(m1.z)));
                const float inv = 1.0f / LL;
                const f32x4 oa = up24(g0), ob = up24(g1);
                const float v0 = quad_sum(fmaf(w0, oa[0], w1_ * ob[0])) * inv, v1 = quad_sum(fmaf(w0, oa[1], w1_ * ob[1])) * inv;
                const float v2 = quad_sum(fmaf(w0, oa[2], w1_ * ob[2])) * inv, v3 = quad_sum(fmaf(w0, oa[3], w1_ * ob[3])) * inv;
                af = u32x2{pack_bf16(v0, v1), pack_bf16(v2, v3)};
            } else {
                const int offh = GH_OFF + ((rg2 * 4 + sq) * 4 + lb) * 256 + (8 * w4 + 2 * g) * 8;
                u32x4 gv;
                for (unsigned spins = 0;;) {
                    CBAR();
                    gv = __builtin_amdgcn_raw_buffer_load_b128(rs, offh, 0, SC1);
                    if (__all(pk24_bad(gv, tg) == 0u) || ++spins > SPIN_CAP) break;
                    __builtin_amdgcn_s_sleep(H2_SLEEP);
                }
                const f32x4 hp = up24(gv);
                float zz[4] = {quad_sum(hp[0]), quad_sum(hp[1]), quad_sum(hp[2]), quad_sum(hp[3])};
                const float bb[4] = {bf_lo(b1v.x), bf_hi(b1v.x), bf_lo(b1v.y), bf_hi(b1v.y)};
#pragma unroll
                for (int e = 0; e < 4; ++e) {
                    const float z = rbf(rbf(zz[e]) + bb[e]);
                    const float u = 0.7978845608028654f * (z + 0.044715f * z * z * z);
                    const float th = 1.0f - 2.0f / (__expf(2.0f * u) + 1.0f);
                    zz[e] = 0.5f * z * (1.0f + th);
                }
                af = u32x2{pack_bf16(zz[0], zz[1]), pack_bf16(zz[2], zz[3])};
            }
            const f32x4 z = {0.f, 0.f, 0.f, 0.f};
            float o8[8];
#pragma unroll
            for (int e = 0; e < 8; ++e) o8[e] = mfma16(af, bfr[e], z)[0];
            float* dst = reinterpret_cast<float*>(smem + L_WP) + (wave * 4 + g) * 128 + c * 8;
            *reinterpret_cast<f32x4*>(dst) = f32x4{o8[0], o8[1], o8[2], o8[3]};
            *reinterpret_cast<f32x4*>(dst + 4) = f32x4{o8[4], o8[5], o8[6], o8[7]};
        }
        __syncthreads();
        if (has2 && w4 < 2) {
            const int t7 = w4 * 64 + lane, r = t7 >> 1, u2 = t7 & 1, wb = wave & 4;
            f32x2 s0 = {0.f, 0.f}, s1 = {0.f, 0.f};
#pragma unroll
            for (int w = 0; w < 4; ++w) {
                s0 += *reinterpret_cast<const f32x2*>(smem + L_WP + (((wb + w) * 4 + 2 * u2) * 128 + 2 * r) * 4);
                s1 += *reinterpret_cast<const f32x2*>(smem + L_WP + (((wb + w) * 4 + 2 * u2 + 1) * 128 + 2 * r) * 4);
            }
            const int cgx = lowh ? cgp : cg2, slot3 = lowh ? rg : 64 + rg2;
            __builtin_amdgcn_raw_buffer_store_b128(pk24(s0.x, s0.y, s1.x, s1.y, tg), rs, G3_OFF + ((cgx * 96 + slot3) * 128 + t7) * 16, 0, SC1);
        }
        if (wave == FINW) {
            const int cgf = j >> 1, rr = (j & 1) * 32 + i;
            const int pp = lane >> 1, u2 = lane & 1;
            const int e = ((lane >> 5) << 1) | ((lane >> 4) & 1), ob = 2 * u2 + (e >> 1), oc = e & 1;
            const float gt = reinterpret_cast<const float*>(smem + L_ST)[ob * 4 + 3];
            const float b2f = oc ? bf_hi(b2v) : bf_lo(b2v);
            const float omg = rbf(1.0f - gt);
            const int off = G3_OFF + (((cgf * 96 + pp) * 64 + rr) * 2 + u2) * 16;
            u32x4 gv[3];
#pragma unroll
            for (int u = 0; u < 3; ++u) gv[u] = u32x4{0u, 0u, 0u, 0u};
            bool ok = false;
            __builtin_amdgcn_s_sleep(H3_DELAY);
            for (unsigned spins = 0;;) {
                CBAR();
                if (!ok) {
#pragma unroll
                    for (int u = 0; u < 3; ++u) gv[u] = __builtin_amdgcn_raw_buffer_load_b128(rs, off + u * (32 * 2048), 0, SC1);
                    u32 bad = pk24_bad(gv[0], tg) | pk24_bad(gv[1], tg);
                    if (pp < 28) bad |= pk24_bad(gv[2], tg);
                    ok = bad == 0u;
                }
                if (__all(ok) || ++spins > SPIN_CAP) break;
                __builtin_amdgcn_s_sleep(H3_SLEEP);
            }
            const f32x4 av = up24(gv[0]) + up24(gv[1]);
            f32x4 cv = up24(gv[2]);
            if (pp >= 28) cv = f32x4{0.f, 0.f, 0.f, 0.f};
            const bool isc = (lane & 2) != 0;
            float v[4];
#pragma unroll
            for (int k = 0; k < 4; ++k) { const float keep = isc ? cv[k] : av[k], give = isc ? av[k] : cv[k]; v[k] = keep + dppf<XOR2>(give); }
            auto sw0 = __builtin_amdgcn_permlane32_swap(__float_as_uint(v[0]), __float_as_uint(v[2]), false, false);
            auto sw1 = __builtin_amdgcn_permlane32_swap(__float_as_uint(v[1]), __float_as_uint(v[3]), false, false);
            const float x0 = __uint_as_float(sw0[0]) + __uint_as_float(sw0[1]), x1 = __uint_as_float(sw1[0]) + __uint_as_float(sw1[1]);
            auto sw2 = __builtin_amdgcn_permlane16_swap(__float_as_uint(x0), __float_as_uint(x1), false, false);
            float y = __uint_as_float(sw2[0]) + __uint_as_float(sw2[1]);
            y = ror_sum(y);
            const float other = dppf<XOR2>(y);
            const float res = rbf(gt * rbf(y)) + rbf(omg * rbf(rbf(other) + b2f));
            if ((lane & 14) == 0) out[ob * 512 + 64 * j + 2 * i + oc] = __builtin_bit_cast(u16, (__bf16)res);
            if (bid == 0 && lane == 0) __builtin_amdgcn_raw_buffer_store_b32(tag, rs, E_OFF, 0, SC1);
        }
    } else {
        const u32 ev = __builtin_amdgcn_raw_buffer_load_b32(rs, E_OFF, 0, SC1);
        if (wave > 8) {
            const int aw = wave - 9, ta = j * 14 + i, kq = ta & 3, cb = ta >> 2, r0 = 128 * kq + 32 * aw;
            const u32x4 xrow = *reinterpret_cast<const u32x4*>(x + aw * 512 + lane * 8);
            const u32x4 gwv = *reinterpret_cast<const u32x4*>(gw + lane * 8);
            const u32x2 xa = *reinterpret_cast<const u32x2*>(x + (c >> 2) * 512 + r0 + (g * 2 + ((c >> 1) & 1)) * 4);
            const __amdgpu_buffer_rsrc_t r1 = __builtin_amdgcn_make_buffer_rsrc((void*)w1, 0, 512 * 1792 * 2, 0x00020000);
            const int off1 = setA ? (r0 + (g * 2 + (c >> 3)) * 4) * 3584 + (64 * cb + (c & 7) * 8) * 2 : OOB;
            u32x4 t2[4];
#pragma unroll
            for (int t = 0; t < 4; ++t) t2[t] = __builtin_amdgcn_raw_buffer_load_b128(r1, off1 + t * 3584, 0, NT);
            SB();
            __syncthreads();
            SB();
            float f[8];
#pragma unroll
            for (int e = 0; e < 4; ++e) { f[2 * e] = bf_lo(xrow[e]); f[2 * e + 1] = bf_hi(xrow[e]); }
            const float mean = wave_sum(((f[0] + f[1]) + (f[2] + f[3])) + ((f[4] + f[5]) + (f[6] + f[7]))) * (1.0f / 512.0f);
            float vs = 0.f, gsum = 0.f;
#pragma unroll
            for (int e = 0; e < 4; ++e) {
                const float d0 = f[2 * e] - mean, d1 = f[2 * e + 1] - mean; vs = fmaf(d0, d0, vs); vs = fmaf(d1, d1, vs);
                gsum += rbf(f[2 * e] * bf_lo(gwv[e])); gsum += rbf(f[2 * e + 1] * bf_hi(gwv[e]));
            }
            const float var = wave_sum(vs) * (1.0f / 512.0f);
            const float gg = rbf(wave_sum(gsum));
            const float sd = rbf(sqrtf(rbf(rbf(var) + 1e-5f)));
            if (lane == 0) *reinterpret_cast<f32x4*>(smem + L_ST + aw * 16) = f32x4{rbf(mean), sd, 1.0f / sd, rbf(1.0f / (1.0f + __expf(-gg)))};
            LDS_FENCE();
            if (lane == 0) __hip_atomic_fetch_add(cnt + 2, 1u, __ATOMIC_RELAXED, __HIP_MEMORY_SCOPE_WORKGROUP);
            if (setA) {
                while (LDS_LD(cnt + 2) < 4u) __builtin_amdgcn_s_sleep(1);
                CBAR();
                const f32x4 stt = *reinterpret_cast<const f32x4*>(smem + L_ST + (c >> 2) * 16);
                float d[4] = {rbf(bf_lo(xa.x) - stt[0]), rbf(bf_hi(xa.x) - stt[0]), rbf(bf_lo(xa.y) - stt[0]), rbf(bf_hi(xa.y) - stt[0])};
#pragma unroll
                for (int e = 0; e < 4; ++e) { const float q0 = d[e] * stt[2]; d[e] = fmaf(fmaf(-q0, stt[1], d[e]), stt[2], q0); }
                const u32 tag = __builtin_amdgcn_readfirstlane(ev) + 1u;
                tile_phase(smem, rs, t2, u32x2{pack_bf16(d[0], d[1]), pack_bf16(d[2], d[3])}, 4 + aw, 1, GH_OFF + (cb * 4 + kq) * 1024, make_tag2(tag), lane);
            }
            return;
        }
        if (lane < 8) cnt[lane] = 0u;
        SB();
        __syncthreads();
        SB();
        const u32 tag = __builtin_amdgcn_readfirstlane(ev) + 1u;
        const Tag2 tg = make_tag2(tag);
        while (LDS_LD(cnt + 0) < 5u) __builtin_amdgcn_s_sleep(1);
        const unsigned long long tpub = __builtin_amdgcn_s_memrealtime();
        while (LDS_LD(cnt + 5) < 1u) __builtin_amdgcn_s_sleep(1);
        while (__builtin_amdgcn_s_memrealtime() - tstart < QDELAY || __builtin_amdgcn_s_memrealtime() - tpub < PDELAY) __builtin_amdgcn_s_sleep(1);
        const int off = GQ_OFF + (((j * 8 + (lane >> 3)) * 4) * 4 + ab) * 256 + (lane & 7) * 32;
        for (unsigned spins = 0;; ++spins) {
            CBAR();
            u32x4 pa[4], pb[4];
#pragma unroll
            for (int k = 0; k < 4; ++k) { pa[k] = __builtin_amdgcn_raw_buffer_load_b128(rs, off + k * 1024, 0, SC1); pb[k] = __builtin_amdgcn_raw_buffer_load_b128(rs, off + k * 1024 + 16, 0, SC1); }
            SB();
            if (spins < 2 && lane == 0) __hip_atomic_store(cnt + 6, spins + 1u, __ATOMIC_RELAXED, __HIP_MEMORY_SCOPE_WORKGROUP);
            SB();
            u32 bad = 0;
#pragma unroll
            for (int k = 0; k < 4; ++k) bad |= pk24_bad(pa[k], tg) | pk24_bad(pb[k], tg);
            if (__all(bad == 0u) || spins > SPIN_CAP) {
                const f32x4 qa = (up24(pa[0]) + up24(pa[1])) + (up24(pa[2]) + up24(pa[3])), qb = (up24(pb[0]) + up24(pb[1])) + (up24(pb[2]) + up24(pb[3]));
                const u32x4 qv = {pack_bf16(qa[0], qa[1]), pack_bf16(qa[2], qa[3]), pack_bf16(qb[0], qb[1]), pack_bf16(qb[2], qb[3])};
                *reinterpret_cast<u32x4*>(smem + L_QS + lane * 16) = qv;
                LDS_FENCE();
                if (lane == 0) { __hip_atomic_store(cnt + 3, 1u, __ATOMIC_RELAXED, __HIP_MEMORY_SCOPE_WORKGROUP); __hip_atomic_store(cnt + 6, 2u, __ATOMIC_RELAXED, __HIP_MEMORY_SCOPE_WORKGROUP); }
                break;
            }
        }
    }
}

extern "C" void kernel_launch(void* const* d_in, const int* in_sizes, int n_in,
                              void* d_out, int out_size, void* d_ws, size_t ws_size,
                              hipStream_t stream) {
    fused_decode<<<256, NTHREADS, 0, stream>>>((const u16*)d_in[0], (const u16*)d_in[1], (const u16*)d_in[2],
                                               (const u16*)d_in[3], (const u16*)d_in[4], (const u16*)d_in[6],
                                               (const u16*)d_in[7], (const u16*)d_in[8], (const u16*)d_in[9],
                                               (const u16*)d_in[10], (u16*)d_out, (unsigned char*)d_ws);
}
```
